```python
import torch

def _softmax(x, axis=-1):
    return torch.softmax(x, dim=axis)
def _gelu(x):
    return torch.nn.functional.gelu(x, approximate="tanh")
def _silu(x):
    return torch.nn.functional.silu(x)
def _sigmoid(x):
    return torch.sigmoid(x)
def _layernorm(x, axis=-1, eps=1e-5):
    m = x.mean(dim=axis, keepdim=True)
    v = x.var(dim=axis, keepdim=True, unbiased=False)
    return (x - m) / torch.sqrt(v + eps)
def _scatter_add(tgt, idx, src, axis=0):
    return tgt.clone().index_add_(axis, idx, src)
def _segment_sum(src, seg, n):
    out = torch.zeros((n,) + src.shape[1:], dtype=src.dtype)
    return out.index_add_(0, seg, src)
def _pad(x, pw):
    flat = tuple(v for pair in reversed(pw) for v in pair)
    return torch.nn.functional.pad(x, flat)

kernel_name = 'recurrent_scan__gqa_block_cached__swiglu_ffn__decode'

def setup_inputs():

    _w = (lambda a: (lambda t: t.to(torch.bfloat16) if t.is_floating_point() else t)(torch.as_tensor(a)))
    def _A_setup():
        B, Sq, D = 1, 1, 576
        Hq, Hkv, Sk, Dh = 32, 16, 8192, 128
        assert Hq % Hkv == 0, (Hq, Hkv)
        return {
            "x": _w((lambda *s: torch.randn(*s, dtype=torch.bfloat16) * 0.05)(B, Sq, D)),
            "k_cache": _w((lambda *s: torch.randn(*s, dtype=torch.bfloat16) * 1.6)(B, Hkv, Sk, Dh)),
            "v_cache": _w((lambda *s: torch.randn(*s, dtype=torch.bfloat16) * 0.2)(B, Hkv, Sk, Dh)),
            "wq": _w((lambda *s: torch.randn(*s, dtype=torch.bfloat16) * 1.6)(D, Hq * Dh) * (D ** -0.5)),
            "wo": _w((lambda *s: torch.randn(*s, dtype=torch.bfloat16) * 0.2)(Hq * Dh, D) * ((Hq * Dh) ** -0.5)),
        }
    _A_in = _A_setup()

    def _B_setup():
        B, S, D, F = 1, 1, 576, 4608
        return {
            "x": _w((lambda *s: torch.randn(*s, dtype=torch.bfloat16) * 0.05)(B, S, D)),
            "w_gate": _w((lambda *s: torch.randn(*s, dtype=torch.bfloat16) * 0.1)(D, F)),
            "w_up": _w((lambda *s: torch.randn(*s, dtype=torch.bfloat16) * 0.1)(D, F)),
            "w_down": _w((lambda *s: torch.randn(*s, dtype=torch.bfloat16) * 0.1)(F, D)),
        }
    _B_in = _B_setup()

    return {
        "A_x": _A_in["x"],
        "A_k_cache": _A_in["k_cache"],
        "A_v_cache": _A_in["v_cache"],
        "A_wq": _A_in["wq"],
        "A_wo": _A_in["wo"],
        "B_x": _B_in["x"],
        "B_w_gate": _B_in["w_gate"],
        "B_w_up": _B_in["w_up"],
        "B_w_down": _B_in["w_down"],
    }

def reference(A_x, A_k_cache, A_v_cache, A_wq, A_wo, B_x, B_w_gate, B_w_up, B_w_down):
    def _A_ref(x, k_cache, v_cache, wq, wo):
        B, Sq, D = x.shape
        Hq, Dh = 32, 128
        Hkv = k_cache.shape[1]
        rep = Hq // Hkv
        q = (lambda x, *d: x.permute(*d))((x @ wq).reshape(B, Sq, Hq, Dh), 0, 2, 1, 3)
        k = (lambda x, n, axis: torch.repeat_interleave(x, n, dim=axis))(k_cache, rep, axis=1)
        v = (lambda x, n, axis: torch.repeat_interleave(x, n, dim=axis))(v_cache, rep, axis=1)
        scores = torch.einsum("bhqd,bhkd->bhqk", q, k) * (Dh ** -0.5)
        p = _softmax(scores, axis=-1)
        o = torch.einsum("bhqk,bhkd->bhqd", p, v)
        return (lambda x, *d: x.permute(*d))(o, 0, 2, 1, 3).reshape(B, Sq, Hq * Dh) @ wo

    def _B_ref(x, w_gate, w_up, w_down):
        g = _silu(x @ w_gate)
        u = x @ w_up
        return (g * u) @ w_down

    x = A_x
    xn = _layernorm(x, axis=-1)
    s = _A_ref(xn, A_k_cache, A_v_cache, A_wq, A_wo)
    g = _silu(_B_ref(xn, B_w_gate, B_w_up, B_w_down))
    return x + s * g
```

```cpp
#include <hip/hip_runtime.h>
#include <hip/hip_bf16.h>
#include <stdint.h>

typedef unsigned v4u __attribute__((ext_vector_type(4)));
typedef float v4f __attribute__((ext_vector_type(4)));
typedef __bf16 bf16x8 __attribute__((ext_vector_type(8)));

constexpr int D = 576, HQ = 32, HKV = 16, SK = 8192, DH = 128, F = 4608;
constexpr int NSPLIT = 16, KEYS = SK / NSPLIT;

__device__ __forceinline__ float bf2f(unsigned short h) { return __uint_as_float((unsigned)h << 16); }
__device__ __forceinline__ unsigned cvtpk(float lo, float hi) {
    unsigned r; asm("v_cvt_pk_bf16_f32 %0, %1, %2" : "=v"(r) : "v"(lo), "v"(hi)); return r;
}
__device__ __forceinline__ float rbf(float x) { return __uint_as_float(cvtpk(x, x) << 16); }
__device__ __forceinline__ v4u ldnt(const void* p) { return __builtin_nontemporal_load((const v4u*)p); }
__device__ __forceinline__ v4f mfma(v4u a, v4u b, v4f c) {
    return __builtin_amdgcn_mfma_f32_16x16x32_bf16(__builtin_bit_cast(bf16x8, a), __builtin_bit_cast(bf16x8, b), c, 0, 0, 0);
}
__device__ __forceinline__ float wave_sum(float v) {
    for (int o = 32; o; o >>= 1) v += __shfl_xor(v, o);
    return v;
}

__device__ __forceinline__ void layernorm(const unsigned short* x, float* xn, int tid, int nthr) {
    const int lane = tid & 63;
    float v[9], s = 0.f;
#pragma unroll
    for (int i = 0; i < 9; ++i) { v[i] = bf2f(x[lane + 64 * i]); s += v[i]; }
    const float m = wave_sum(s) * (1.f / D);
    float q = 0.f;
#pragma unroll
    for (int i = 0; i < 9; ++i) q += (v[i] - m) * (v[i] - m);
    const float var = wave_sum(q) * (1.f / D);
    const float r = 1.f / sqrtf(var + 1e-5f);
    if (tid < 64) {
#pragma unroll
        for (int i = 0; i < 9; ++i) xn[lane + 64 * i] = rbf((v[i] - m) * r);
    }
}

template <int NLD>
__device__ __forceinline__ v4f gemv_tile(const unsigned short* W, int ldw, const float* vec, int lane) {
    v4u w[NLD];
    const unsigned short* p = W + (size_t)(2 * (lane >> 4) + ((lane >> 3) & 1)) * ldw + 8 * (lane & 7);
#pragma unroll
    for (int i = 0; i < NLD; ++i) w[i] = ldnt(p + (size_t)8 * i * ldw);
    unsigned msk[4];
#pragma unroll
    for (int k = 0; k < 4; ++k) msk[k] = ((lane & 7) >> 1) == k ? ((lane & 1) ? 0xffff0000u : 0x0000ffffu) : 0u;
    v4f acc = {0.f, 0.f, 0.f, 0.f};
#pragma unroll
    for (int i = 0; i < NLD; ++i) {
        const float s = vec[8 * i + 2 * (lane >> 4) + ((lane >> 3) & 1)];
        const unsigned b = cvtpk(s, s);
        v4u bb = {b & msk[0], b & msk[1], b & msk[2], b & msk[3]};
        acc = mfma(w[i], bb, acc);
    }
    return acc;
}

__global__ void __launch_bounds__(512) k1(const unsigned short* x, const unsigned short* wq, const unsigned short* wg,
                                         const unsigned short* wu, float* y) {
    __shared__ float xn[D];
    __shared__ float red[8][256];
    const int tid = threadIdx.x, lane = tid & 63, wave = tid >> 6, b = blockIdx.x;
    const unsigned short* W; int ldw, col0;
    if (b < 64) { W = wq; ldw = HQ * DH; col0 = b * 64; }
    else if (b < 136) { W = wg; ldw = F; col0 = (b - 64) * 64; }
    else { W = wu; ldw = F; col0 = (b - 136) * 64; }
    layernorm(x, xn, tid, 512);
    __syncthreads();
    v4f acc = gemv_tile<9>(W + (size_t)wave * 72 * ldw + col0, ldw, xn + wave * 72, lane);
    *(v4f*)&red[wave][lane * 4] = acc;
    __syncthreads();
    if (tid < 64) {
        const int r = tid >> 3, c = tid & 7;
        float s = 0.f;
#pragma unroll
        for (int w = 0; w < 8; ++w)
            s += red[w][((r >> 2) * 16 + c) * 4 + (r & 3)] + red[w][(((r + 8) >> 2) * 16 + c + 8) * 4 + (r & 3)];
        y[b * 64 + tid] = s;
    }
}

__global__ void __launch_bounds__(1024) k2(const unsigned short* kc, const unsigned short* vc, const float* y, float* part) {
    __shared__ float red[16][256];
    __shared__ float ml[16][4];
    const int tid = threadIdx.x, lane = tid & 63, wave = tid >> 6, b = blockIdx.x;
    const int h = b >> 4, sp = b & 15;
    const size_t base = ((size_t)h * SK + sp * KEYS + wave * 32) * DH;
    v4u K[8], V[8];
#pragma unroll
    for (int t = 0; t < 2; ++t)
#pragma unroll
        for (int i = 0; i < 4; ++i) K[t * 4 + i] = ldnt(kc + base + (size_t)(t * 16 + (lane & 15)) * DH + 32 * i + 8 * (lane >> 4));
#pragma unroll
    for (int t = 0; t < 2; ++t)
#pragma unroll
        for (int i = 0; i < 4; ++i) V[t * 4 + i] = ldnt(vc + base + (size_t)(t * 16 + 4 * (lane >> 4) + i) * DH + 8 * (lane & 15));
    v4u q[4];
    {
        const float* qp = y + (2 * h + ((lane >> 3) & 1)) * DH + 8 * (lane >> 4);
#pragma unroll
        for (int i = 0; i < 4; ++i) {
            const v4f a = *(const v4f*)(qp + 32 * i), c = *(const v4f*)(qp + 32 * i + 4);
            q[i] = v4u{cvtpk(a.x, a.y), cvtpk(a.z, a.w), cvtpk(c.x, c.y), cvtpk(c.z, c.w)};
        }
    }
    constexpr float C2 = 0.088388347648318440f * 1.4426950408889634f;
    v4f s[2];
#pragma unroll
    for (int t = 0; t < 2; ++t) {
        s[t] = v4f{0.f, 0.f, 0.f, 0.f};
#pragma unroll
        for (int i = 0; i < 4; ++i) s[t] = mfma(K[t * 4 + i], q[i], s[t]);
    }
    float m = fmaxf(fmaxf(fmaxf(s[0].x, s[0].y), fmaxf(s[0].z, s[0].w)), fmaxf(fmaxf(s[1].x, s[1].y), fmaxf(s[1].z, s[1].w)));
    m = fmaxf(m, __shfl_xor(m, 16)); m = fmaxf(m, __shfl_xor(m, 32));
    m *= C2;
    unsigned msk[4];
#pragma unroll
    for (int k = 0; k < 4; ++k) msk[k] = ((lane & 7) >> 1) == k ? ((lane & 1) ? 0xffff0000u : 0x0000ffffu) : 0u;
    float l = 0.f;
    v4f o = {0.f, 0.f, 0.f, 0.f};
#pragma unroll
    for (int t = 0; t < 2; ++t)
#pragma unroll
        for (int i = 0; i < 4; ++i) {
            const float p = __builtin_amdgcn_exp2f(s[t][i] * C2 - m);
            const unsigned pb = cvtpk(p, p);
            l += __uint_as_float(pb << 16);
            v4u bb = {pb & msk[0], pb & msk[1], pb & msk[2], pb & msk[3]};
            o = mfma(V[t * 4 + i], bb, o);
        }
    l += __shfl_xor(l, 16); l += __shfl_xor(l, 32);
    *(v4f*)&red[wave][lane * 4] = o;
    if (lane == 0 || lane == 8) { ml[wave][(lane >> 3) * 2] = m; ml[wave][(lane >> 3) * 2 + 1] = l; }
    __syncthreads();
    if (tid < 256) {
        const int hd = tid >> 7, d = tid & 127, r = d >> 3, c = (d & 7) + 8 * hd;
        float M = -INFINITY;
#pragma unroll
        for (int w = 0; w < 16; ++w) M = fmaxf(M, ml[w][hd * 2]);
        float L = 0.f, acc = 0.f;
#pragma unroll
        for (int w = 0; w < 16; ++w) {
            const float f = __builtin_amdgcn_exp2f(ml[w][hd * 2] - M);
            L += f * ml[w][hd * 2 + 1];
            acc += f * red[w][((r >> 2) * 16 + c) * 4 + (r & 3)];
        }
        float* pp = part + ((size_t)(2 * h + hd) * NSPLIT + sp) * 132;
        pp[4 + d] = acc;
        if (d == 0) { pp[0] = M; pp[1] = L; }
    }
}

__global__ void __launch_bounds__(512) k3(const unsigned short* wo, const unsigned short* wd, const float* y, const float* part, float* p2) {
    __shared__ float o[256];
    __shared__ float hh[320];
    __shared__ float red[2][8][256];
    const int tid = threadIdx.x, lane = tid & 63, wave = tid >> 6, b = blockIdx.x;
    const int h = b / 9, ct = b % 9;
    if (tid < 256) {
        const int hd = tid >> 7, d = tid & 127;
        const float* pp = part + (size_t)(2 * h + hd) * NSPLIT * 132;
        float M = -INFINITY;
#pragma unroll
        for (int s = 0; s < NSPLIT; ++s) M = fmaxf(M, pp[s * 132]);
        float L = 0.f, acc = 0.f;
#pragma unroll
        for (int s = 0; s < NSPLIT; ++s) {
            const float f = __builtin_amdgcn_exp2f(pp[s * 132] - M);
            L += f * pp[s * 132 + 1];
            acc += f * pp[s * 132 + 4 + d];
        }
        o[tid] = acc / L;
    }
    if (tid < 320) {
        float v = 0.f;
        if (tid < 288) {
            const float g = rbf(y[4096 + h * 288 + tid]), u = rbf(y[4096 + F + h * 288 + tid]);
            v = rbf(g / (1.f + __expf(-g))) * u;
        }
        hh[tid] = v;
    }
    __syncthreads();
    v4f a = gemv_tile<4>(wo + (size_t)(h * 256 + wave * 32) * D + ct * 64, D, o + wave * 32, lane);
    *(v4f*)&red[0][wave][lane * 4] = a;
    v4f f = {0.f, 0.f, 0.f, 0.f};
    if (wave < 7) f = gemv_tile<5>(wd + (size_t)(h * 288 + wave * 40) * D + ct * 64, D, hh + wave * 40, lane);
    else f = gemv_tile<1>(wd + (size_t)(h * 288 + 280) * D + ct * 64, D, hh + 280, lane);
    *(v4f*)&red[1][wave][lane * 4] = f;
    __syncthreads();
    if (tid < 128) {
        const int z = tid >> 6, r = (tid >> 3) & 7, c = tid & 7;
        float s = 0.f;
#pragma unroll
        for (int w = 0; w < 8; ++w)
            s += red[z][w][((r >> 2) * 16 + c) * 4 + (r & 3)] + red[z][w][(((r + 8) >> 2) * 16 + c + 8) * 4 + (r & 3)];
        p2[(z * 16 + h) * D + ct * 64 + (tid & 63)] = s;
    }
}

__global__ void __launch_bounds__(64) k4(const unsigned short* x, const float* p2, unsigned short* out) {
    const int j = blockIdx.x * 64 + threadIdx.x;
    float s = 0.f, f = 0.f;
#pragma unroll
    for (int h = 0; h < 16; ++h) { s += p2[h * D + j]; f += p2[(16 + h) * D + j]; }
    s = rbf(s); f = rbf(f);
    const float g = rbf(f / (1.f + __expf(-f)));
    const float r = bf2f(x[j]) + rbf(s * g);
    out[j] = (unsigned short)(cvtpk(r, r) & 0xffff);
}

extern "C" void kernel_launch(void* const* d_in, const int* in_sizes, int n_in,
                              void* d_out, int out_size, void* d_ws, size_t ws_size,
                              hipStream_t stream) {
    const unsigned short* x = (const unsigned short*)d_in[0];
    const unsigned short* kc = (const unsigned short*)d_in[1];
    const unsigned short* vc = (const unsigned short*)d_in[2];
    const unsigned short* wq = (const unsigned short*)d_in[3];
    const unsigned short* wo = (const unsigned short*)d_in[4];
    const unsigned short* wg = (const unsigned short*)d_in[6];
    const unsigned short* wu = (const unsigned short*)d_in[7];
    const unsigned short* wd = (const unsigned short*)d_in[8];
    float* y = (float*)d_ws;
    float* part = y + 16384;
    float* p2 = part + HQ * NSPLIT * 132;
    k1<<<208, 512, 0, stream>>>(x, wq, wg, wu, y);
    k2<<<256, 1024, 0, stream>>>(kc, vc, y, part);
    k3<<<144, 512, 0, stream>>>(wo, wd, y, part, p2);
    k4<<<9, 64, 0, stream>>>(x, p2, (unsigned short*)d_out);
}
```

```cpp
#include <hip/hip_runtime.h>
#include <hip/hip_bf16.h>
#include <stdint.h>

typedef unsigned v4u __attribute__((ext_vector_type(4)));
typedef unsigned v8u __attribute__((ext_vector_type(8)));
typedef unsigned v2u __attribute__((ext_vector_type(2)));
typedef float v4f __attribute__((ext_vector_type(4)));
typedef float v2f __attribute__((ext_vector_type(2)));
typedef __bf16 bf16x2 __attribute__((ext_vector_type(2)));
typedef __bf16 bf16x8 __attribute__((ext_vector_type(8)));
typedef __amdgpu_buffer_rsrc_t rsrc_t;
#define LAS __attribute__((address_space(3)))

constexpr int WIN = 4;
constexpr int XP = 7, QCHK = 18;
constexpr int R0 = 30, RB = 28, TA = 142;
constexpr int N1 = 5, TB1 = 285, TB10 = 275;
constexpr int TQ = 564, TH = 852;
constexpr int QSL = 8, PRE = 8, PRE2 = 50, DSL = 4;
constexpr int OPLAN = 0x1233, DPLAN = 0x1234;

constexpr int D = 576, F = 4608;
constexpr int G1_OFF = 4096;
constexpr int ML_OFF = 57344;
constexpr int P1_OFF = 65536;
constexpr int P2_OFF = P1_OFF + 262144;
constexpr int WS_BYTES = P2_OFF + 144 * 1024;
constexpr int OOB = 0x7ffffff0;
constexpr int NT = 2, SC1 = 16;
constexpr unsigned SPIN = 1u << 16;
constexpr float LOG2E = 1.4426950408889634f;
constexpr float C2 = 0.088388347648318440f * LOG2E;

__host__ __device__ constexpr int map_h(int b) { return (b & 7) + 8 * (b >> 7); }
__host__ __device__ constexpr int map_sp(int b) { return (b >> 3) & 15; }

struct Args {
    const char *x, *wq, *wg, *wu, *k, *v, *wo, *wd;
    char *ws, *out;
};

__device__ __forceinline__ float asf(unsigned u) { return __uint_as_float(u); }
__device__ __forceinline__ unsigned asu(float f) { return __float_as_uint(f); }
__device__ __forceinline__ float lo16(unsigned u) { return asf(u << 16); }
__device__ __forceinline__ float hi16(unsigned u) { return asf(u & 0xffff0000u); }
__device__ __forceinline__ unsigned cvtpk(float lo, float hi) {
    return __builtin_bit_cast(unsigned, __builtin_convertvector(v2f{lo, hi}, bf16x2));
}
__device__ __forceinline__ float rbf(float x) { return lo16(cvtpk(x, x)); }
__device__ __forceinline__ v4f mfma(v4u a, v4u b, v4f c) {
    return __builtin_amdgcn_mfma_f32_16x16x32_bf16(__builtin_bit_cast(bf16x8, a), __builtin_bit_cast(bf16x8, b), c, 0, 0, 0);
}
template <int C> __device__ __forceinline__ float dpp(float v) { return asf(__builtin_amdgcn_mov_dpp(asu(v), C, 0xf, 0xf, true)); }
template <int C> __device__ __forceinline__ unsigned dppu(unsigned v) { return __builtin_amdgcn_mov_dpp(v, C, 0xf, 0xf, true); }
constexpr int X1 = 0xB1, X2 = 0x4E, HMIR = 0x141, MIR = 0x140, ROR4 = 0x124, ROR8 = 0x128;
__device__ __forceinline__ float add16(float a, float b) { auto r = __builtin_amdgcn_permlane16_swap(asu(a), asu(b), false, false); return asf(r[0]) + asf(r[1]); }
__device__ __forceinline__ float add16(float v) { return add16(v, v); }
__device__ __forceinline__ float add32(float v) { auto r = __builtin_amdgcn_permlane32_swap(asu(v), asu(v), false, false); return asf(r[0]) + asf(r[1]); }
__device__ __forceinline__ float max16(float v) { auto r = __builtin_amdgcn_permlane16_swap(asu(v), asu(v), false, false); return fmaxf(asf(r[0]), asf(r[1])); }
__device__ __forceinline__ float max32(float v) { auto r = __builtin_amdgcn_permlane32_swap(asu(v), asu(v), false, false); return fmaxf(asf(r[0]), asf(r[1])); }
__device__ __forceinline__ float sum8(float v) { v += dpp<X1>(v); v += dpp<X2>(v); v += dpp<HMIR>(v); return v; }
__device__ __forceinline__ float max8(float v) { v = fmaxf(v, dpp<X1>(v)); v = fmaxf(v, dpp<X2>(v)); return fmaxf(v, dpp<HMIR>(v)); }
__device__ __forceinline__ float sum64(float v) { v = sum8(v); v += dpp<MIR>(v); return add32(add16(v)); }
__device__ __forceinline__ float silu(float g) { return g * __builtin_amdgcn_rcpf(1.f + __builtin_amdgcn_exp2f(-LOG2E * g)); }
__device__ __forceinline__ rsrc_t mkrs(const void* p, int bytes) { return __builtin_amdgcn_make_buffer_rsrc((void*)p, 0, bytes, 0x00020000); }
template <int AUX> __device__ __forceinline__ v4u ld(rsrc_t r, int vo, int so = 0) { return __builtin_amdgcn_raw_buffer_load_b128(r, vo, so, AUX); }
__device__ __forceinline__ void put(rsrc_t r, int vo, unsigned a, unsigned t0, unsigned b, unsigned t1) {
    __builtin_amdgcn_raw_buffer_store_b128(v4u{a, t0, b, t1}, r, vo, 0, SC1);
}
template <typename T> __device__ __forceinline__ T sel(bool c, T a, T b) { return c ? a : b; }
template <int I> struct Idx { static constexpr int v = I; };
template <int I, int N, typename Fn> __device__ __forceinline__ void steps(Fn&& f) {
    if constexpr (I < N) { f(Idx<I>{}); steps<I + 1, N>(f); }
}
#define STEP(n) [&](auto n##_) __attribute__((always_inline))
#define USE(x) asm volatile("" ::"v"(x))
#define KEEPV(x) asm volatile("" : "+v"(x))
#define KEEPS(x) asm volatile("" : "+s"(x))
#define FRESH() asm volatile("" ::: "memory")
#define PIN() __builtin_amdgcn_sched_barrier(0)
#define BAD(g) (((g).y ^ T0) | ((g).w ^ T1))
#define ANY(x) __builtin_expect(__builtin_amdgcn_ballot_w64((x) != 0u) != 0ull, 0)

struct Smem {
    v4f red1[8][64], red2[8][64], red3[3][8][64];
    v4u xn[144];
    v4f hb[80];
    unsigned cnt[2][64];
    unsigned qf[64], xf[64];
    v2u q[64];
    v2f mw[2][8];
    float fw[2][8];
    v2u tag;
};

__device__ __forceinline__ void arrive(unsigned* c, int lane) {
    FRESH();
    __hip_atomic_fetch_add(c + lane, 1u, __ATOMIC_RELAXED, __HIP_MEMORY_SCOPE_WORKGROUP);
    FRESH();
}
__device__ __forceinline__ void await8(const unsigned* c, int lane) {
#pragma nounroll
    for (unsigned s = 0; s < SPIN * 64; ++s) {
        FRESH();
        const unsigned n = c[lane];
        FRESH();
        if (__builtin_amdgcn_readfirstlane(n) == 8) break;
        __builtin_amdgcn_s_sleep(1);
    }
}
#define BARRIER() asm volatile("s_waitcnt lgkmcnt(0)\n s_barrier" ::: "memory")
template <int STEP2> __device__ __forceinline__ float tile_sum(const float* p) {
    float a = 0.f, b = 0.f;
#pragma unroll
    for (int w = 0; w < 8; ++w) { a += p[w * 256]; b += p[w * 256 + STEP2]; }
    return a + b;
}
__device__ __forceinline__ unsigned now() { return (unsigned)__builtin_amdgcn_s_memrealtime(); }
__device__ __forceinline__ void until(unsigned t0, unsigned dt) {
#pragma nounroll
    for (unsigned i = 0; i < SPIN; ++i) {
        if (now() - t0 >= dt) break;
        __builtin_amdgcn_s_sleep(2);
    }
}

__global__ void __launch_bounds__(576, 3) __attribute__((aligned(4096))) fused_decode(Args a) {
    __shared__ Smem sm;
    asm volatile("" ::: "v160");
    unsigned long long t64 = __builtin_amdgcn_s_memrealtime();
    const int tid = threadIdx.x, lane = tid & 63, w = __builtin_amdgcn_readfirstlane(tid >> 6), b = blockIdx.x;
    const int h = map_h(b), sp = map_sp(b);
    const int r = lane >> 3, j = lane & 7;
    const bool isQ = sp < 4, has1 = sp <= 12, isO = !has1, isD = has1 && !isQ;
    const int gi = 9 * h + sp - 4;

    unsigned zc0 = 0, zc1 = 0;
    {
        unsigned long long pc; unsigned n;
        asm volatile("s_cmp_lg_u32 %5, 8\n s_cbranch_scc1 1f\n s_getpc_b64 %2\n.Lcode_here:\n s_mov_b32 %3, .Lcode_last-.Lcode_here\n"
                     "global_load_dword %0, %4, %2\n v_or_b32 %1, 0x2000, %4\n v_min_u32 %1, %3, %1\n global_load_dword %1, %1, %2\n1:"
                     : "+v"(zc0), "+v"(zc1), "=&s"(pc), "=&s"(n) : "v"(lane * 128), "s"(w) : "scc");
    }
    if (w == 8) {
        const rsrc_t rx = mkrs(a.x, D * 2), rw = mkrs(a.ws, WS_BYTES);
        const v4u xa = ld<0>(rx, lane * 16);
        const unsigned xb = __builtin_amdgcn_raw_buffer_load_b32(rx, 1024 + lane * 4, 0, 0);
        const v2u ep = __builtin_amdgcn_raw_buffer_load_b64(rw, 0, 0, SC1);
        KEEPS(t64);
        const unsigned tb = (unsigned)t64;
        sm.cnt[0][lane] = 0; sm.cnt[1][lane] = 0; sm.qf[lane] = 0; sm.xf[lane] = 0;
        BARRIER();
        PIN();
        const unsigned z = __builtin_amdgcn_raw_buffer_load_b32(rw, lane < 15 ? G1_OFF + (b * 15 + lane) * 128 : OOB, 0, SC1);
        PIN();

        const float e[10] = {lo16(xa.x), hi16(xa.x), lo16(xa.y), hi16(xa.y), lo16(xa.z), hi16(xa.z), lo16(xa.w), hi16(xa.w), lo16(xb), hi16(xb)};
        float s = 0.f;
#pragma unroll
        for (int i = 0; i < 10; ++i) s += e[i];
        const float mean = sum64(s) * (1.f / D);
        float c[10];
#pragma unroll
        for (int i = 0; i < 10; ++i) c[i] = e[i] - mean;
        float q = c[8] * c[8] + c[9] * c[9];
        q = lane < 32 ? q : 0.f;
#pragma unroll
        for (int i = 0; i < 8; ++i) q += c[i] * c[i];
        const float rs = __builtin_amdgcn_rsqf(sum64(q) * (1.f / D) + 1e-5f);
        unsigned n[10];
#pragma unroll
        for (int i = 0; i < 10; ++i) { const float t = c[i] * rs; n[i] = cvtpk(t, t); }
        sm.xn[2 * lane] = v4u{n[0], n[1], n[2], n[3]};
        sm.xn[2 * lane + 1] = v4u{n[4], n[5], n[6], n[7]};
        if (lane < 32) ((v2u*)&sm.xn[128])[lane] = v2u{n[8], n[9]};
        const unsigned T0 = __builtin_amdgcn_readfirstlane((ep.x + 0x9E3779B1u) | 1u), T1 = __builtin_amdgcn_readfirstlane(ep.y + 0x85EBCA77u);
        if (lane == 0) sm.tag = v2u{T0, T1};
        FRESH();
        sm.xf[lane] = 1;
        FRESH();

        USE(z);
        asm volatile("s_waitcnt vmcnt(0)" : "+v"(zc0), "+v"(zc1));

        v4u g;
        const int oq = G1_OFF + (64 * h + lane) * 16;
        bool asked = false;
        if (has1) {
#pragma nounroll
            for (unsigned i = 0; i < SPIN * 64; ++i) {
                FRESH();
                const unsigned n = sm.cnt[0][lane];
                FRESH();
                if (__builtin_amdgcn_readfirstlane(n) == 8) break;
                if (isD && !asked && now() - tb >= TQ) { g = ld<SC1>(rw, oq); asked = true; }
                __builtin_amdgcn_s_sleep(1);
            }
            const float y = tile_sum<160>((const float*)sm.red1 + ((lane >> 5) * 16 + j) * 4 + (r & 3));
            const unsigned pk = cvtpk(y, dpp<X1>(y));
            put(rw, (lane & 3) == 0 ? G1_OFF + (sel(isQ, 256 * h + 64 * sp, 4096 + gi * 64) + lane) * 4 : OOB, pk, T0, dppu<0xAA>(pk), T1);
        }
        if (!asked) { until(tb, TQ); g = ld<SC1>(rw, oq); }
#pragma nounroll
        for (unsigned i = 0; i < SPIN; ++i) {
            if (!ANY(BAD(g))) break;
            __builtin_amdgcn_s_sleep(QSL);
            FRESH();
            g = ld<SC1>(rw, oq);
        }
        sm.q[lane] = v2u{g.x, g.z};
        FRESH();
        sm.qf[lane] = 1;
        FRESH();

        await8(sm.cnt[1], lane);
        {
            const v2f mz = sm.mw[r & 1][j];
            const float M = max8(mz.x), f = __builtin_amdgcn_exp2f(mz.x - M);
            put(rw, (lane & 55) == 0 ? ML_OFF + ((h * 2 + r) * 16 + sp) * 16 : OOB, asu(M), T0, asu(sum8(f * mz.y)), T1);
            if (lane < 16) sm.fw[r][j] = f;
        }
        if (isD) {
            const int o0 = G1_OFF + (1024 + 72 * h + lane) * 16, o1 = lane < 8 ? o0 + 1024 : OOB;
            const unsigned lm = lane < 8 ? ~0u : 0u;
            v4u g0, u0, g1, u1;
            until(tb, TH);
#pragma nounroll
            for (unsigned i = 0; i < SPIN; ++i) {
                FRESH();
                g0 = ld<SC1>(rw, o0); u0 = ld<SC1>(rw, o0, (F / 4) * 16);
                g1 = ld<SC1>(rw, o1); u1 = ld<SC1>(rw, o1, (F / 4) * 16);
                if (!ANY(BAD(g0) | BAD(u0) | ((BAD(g1) | BAD(u1)) & lm))) break;
                __builtin_amdgcn_s_sleep(QSL);
            }
#define HID(g, u) v4f{rbf(silu(lo16(g.x))) * lo16(u.x), rbf(silu(hi16(g.x))) * hi16(u.x), rbf(silu(lo16(g.z))) * lo16(u.z), rbf(silu(hi16(g.z))) * hi16(u.z)}
            sm.hb[lane] = HID(g0, u0);
            if (lane < 16) sm.hb[64 + lane] = HID(g1, u1);
        }
        FRESH();
        const v4f fa = *(const v4f*)&sm.fw[r & 1][0], fb = *(const v4f*)&sm.fw[r & 1][4];
        int op1 = P1_OFF + ((h * 2 + (r & 1)) * 8 + (r & 6) + (j >> 2)) * 1024 + (sp * 4 + (j & 3)) * 16;
        KEEPV(op1);
        USE(fa); USE(fb);
        __builtin_amdgcn_s_setprio(3);
        BARRIER();
        v4f p[8];
#pragma unroll
        for (int i = 0; i < 8; ++i) p[i] = sm.red2[i][lane];
        PIN();
        v4f o = fa.x * p[0] + fa.y * p[1] + fa.z * p[2] + fa.w * p[3];
        o += fb.x * p[4] + fb.y * p[5] + fb.z * p[6] + fb.w * p[7];
        put(rw, op1, cvtpk(o.x, o.y), T0, cvtpk(o.z, o.w), T1);
        asm volatile(".Lcode_last:");
        __builtin_amdgcn_endpgm();
    }

    const bool isU = gi >= 72;
    const int c1 = sel(isQ, 4 * h + sp, sel(isU, gi - 72, gi));
    const int ld1 = sel(isQ, 8192, F * 2);
    const int kvb = w * 1024 + ((sp & 1) << 14 | (sp >> 1) << 18);
#define KST(t) (((t) & 1) << 13 | ((t) >> 1) << 15)
    v4u S1[9], K[16], V[16], S3[5];
#define LD1(i) S1[i] = (i) < 8 ? ld<NT>(r1, o1, ((i) & 3) * sa1 + ((i) >> 2) * sb1) : ld<NT>(r1, o1t)
#define LDK(t) K[t] = ld<NT>(rk, ok_, KST(t))
#define LDV(t) V[t] = ld<NT>(rv, ov, KST(t))
#define LDO(n) S3[n] = ld<NT>(r3, o3, ((n) + 1) * 2 * D * 2)
#define LDD(n) S3[n] = ld<NT>(r3, (n) < 4 ? o3 : o3x, (n) * 8 * D * 2)
#define ISSUE(p) do { if constexpr ((p) < 9) LD1(p); else if constexpr ((p) < 25) LDK((p) - 9); else LDV((p) - 25); } while (0)
#define LANDED(p) do { if constexpr ((p) < 9) USE(S1[p]); else if constexpr ((p) < 25) USE(K[(p) - 9]); else USE(V[(p) - 25]); } while (0)
#define WAITT(t, base) do { unsigned v_; \
        asm volatile("1: s_memrealtime vcc\n s_waitcnt lgkmcnt(0)\n s_sub_u32 %0, vcc_lo, %2\n s_cmp_lt_i32 %0, %1\n s_cbranch_scc1 1b" \
                     : "=&s"(v_) : "n"(t), "s"(base) : "scc", "vcc"); } while (0)
    const int orow = (32 * w + 8 * (r >> 1) + (r & 1)) * (D * 2), offo = 256 * h * (D * 2) + (sp - 13) * 384;
    int o1 = sel(has1, (64 * w + 8 * r) * ld1, orow) + j * 16, o1t = sel(has1, (512 + 8 * w + r) * ld1, orow + 256) + j * 16;
    int off1 = sel(has1, c1 * 128, offo), len1 = sel(has1, (D - 1) * ld1 + 128, 255 * D * 2 + 384);
    int pick1 = sel(isQ, 0, sel(isO, 3, sel(isU, 2, 1)));
    const int sa1 = sel(has1, ld1, 2 * D * 2), sb1 = sel(has1, 4 * ld1, 128);
    const char *pq = a.wq, *pg = a.wg, *pu = a.wu, *po = a.wo;
    asm volatile("" : "+v"(o1));
    asm volatile("" : "+s"(off1), "+s"(len1), "+s"(pick1));
    asm volatile("" : "+s"(pq), "+s"(pg), "+s"(pu), "+s"(po));
    const rsrc_t r1 = mkrs(sel(pick1 < 2, sel(pick1 == 0, pq, pg), sel(pick1 == 2, pu, po)) + (unsigned)off1, len1);
    LD1(0);
    PIN();
    KEEPS(t64);
    const unsigned tb = (unsigned)t64;
    unsigned tn = tb;
    PIN();

#define SCHED(n) (TA + ((n) <= N1 ? TB1 * (n) : TB1 * N1 + TB10 * ((n) - N1)) / 10)
#define PACE(n) do { unsigned v_; \
        asm volatile("s_cmp_ge_i32 %1, %2\n s_cbranch_scc1 2f\n1: s_memrealtime vcc\n s_waitcnt lgkmcnt(0)\n" \
                     "s_sub_u32 %0, vcc_lo, %3\n s_cmp_lt_i32 %0, %2\n s_cbranch_scc1 1b\n2:" \
                     : "=&s"(v_) : "s"(tn - tb), "n"(SCHED(n)), "s"(tb) : "scc", "vcc"); } while (0)
#define TICK() do { PIN(); tn = now(); } while (0)

    const rsrc_t rw = mkrs(a.ws, WS_BYTES);
    const rsrc_t rk = mkrs(a.k + ((unsigned)h << 21), 2 << 20);
    int ok_ = kvb + ((lane >> 2) & 3) * 256 + (lane >> 4) * 64 + (lane & 3) * 16;
    int off3 = sel(isD, 288 * h * (D * 2) + (sp - 4) * 128, offo);
    int len3 = sel(isO, 255 * D * 2 + 384, sel(isD, 287 * D * 2 + 128, 0));
    int o3 = sel(isD, (36 * w + r) * (D * 2) + j * 16, o1t);
    int o3x = r < 4 ? o3 : OOB;
    const int cg = 9 * h + 3 * sp + w;
    int ov = kvb + lane * 16;
    int og1 = P1_OFF + (h * 8 + w) * 2048 + lane * 16;
    const int pr = sel(isD, 16 + h, h);
    int op2 = ((lane ^ pr) & 1) != 0 ? OOB : P2_OFF + (sel(isD, sp - 4, 3 * sp - 39 + w) * 16 + (lane >> 2)) * 1024 + pr * 32 + (lane & 2) * 8;
    int og2 = P2_OFF + cg * 1024 + lane * 16;
    unsigned lowq = (lane & 4) ? 0u : ~0u, highq = ~lowq;
    unsigned msk[4];
#pragma unroll
    for (int k = 0; k < 4; ++k) { msk[k] = (j >> 1) == k ? ((lane & 1) ? 0xffff0000u : 0x0000ffffu) : 0u; KEEPV(msk[k]); }
#define SPARSE(pb) v4u{(pb) & msk[0], (pb) & msk[1], (pb) & msk[2], (pb) & msk[3]}
    KEEPS(off3); KEEPS(len3);
    KEEPV(o1t); KEEPV(ok_); KEEPV(o3); KEEPV(o3x); KEEPV(ov); KEEPV(og1); KEEPV(op2); KEEPV(og2); KEEPV(lowq); KEEPV(highq);
    PIN();
    steps<1, WIN>(STEP(p) { constexpr int p = decltype(p_)::v; WAITT(R0 + p * RB, tb); PIN(); LD1(p); PIN(); });
    const char *pv = a.v, *pd = a.wd, *pout = a.out;
    asm volatile("" : "+s"(pv), "+s"(pd));
    const char* p3 = sel(isD, pd, po) + (unsigned)off3;
    KEEPS(p3);
    const rsrc_t rv = mkrs(pv + ((unsigned)h << 21), 2 << 20);
    const rsrc_t r3 = mkrs(p3, len3);
    PIN();
    BARRIER();
    PIN();

    static_assert(WIN >= 4 && WIN <= 6 && XP < 9 && QCHK - WIN + 9 > 9, "schedule");
    const v4u* const qs = (const v4u*)sm.q + (r & 1) * 16 + (lane >> 4) * 4 + (lane & 3);
    v4u xn0, xn1, qv, qe, qo;
    v2u tg;
    unsigned xn8, xok, qok, T0, T1;
    float sc[8], ta[8], tb_[8];
    v4f acc1 = {0.f, 0.f, 0.f, 0.f};
#define LDX() do { FRESH(); xok = sm.xf[lane]; FRESH(); \
        xn0 = sm.xn[16 * w + 2 * r]; xn1 = sm.xn[16 * w + 2 * r + 1]; xn8 = ((const unsigned*)sm.xn)[512 + 8 * w + r]; tg = sm.tag; FRESH(); } while (0)
    steps<0, 25>(STEP(n) {
        constexpr int n = decltype(n_)::v, QN = QCHK - WIN + 9;
        if constexpr (n == XP) LDX();
        if constexpr (n == QN) { FRESH(); qok = sm.qf[lane]; FRESH(); qv = *qs; FRESH(); }
        LANDED(n);
        PACE(n);
        PIN();
        ISSUE(n + WIN);
        TICK();
        PIN();
        if constexpr (n == XP) {
            if (__builtin_expect(__builtin_amdgcn_readfirstlane(xok) == 0, 0)) {
#pragma nounroll
                for (unsigned s = 0; s < SPIN * 64; ++s) {
                    __builtin_amdgcn_s_sleep(1);
                    LDX();
                    if (__builtin_amdgcn_readfirstlane(xok) != 0) break;
                }
            }
            T0 = __builtin_amdgcn_readfirstlane(tg.x); T1 = __builtin_amdgcn_readfirstlane(tg.y);
            PIN();
        }
        if constexpr (n >= XP && n < 9) {
            const unsigned xn[9] = {xn0.x, xn0.y, xn0.z, xn0.w, xn1.x, xn1.y, xn1.z, xn1.w, xn8};
            constexpr int lo = n == XP ? 0 : n, hi = n + 1;
#pragma unroll
            for (int i = 0; i < 9; ++i) if (i >= lo && i < hi) { acc1 = mfma(S1[i], SPARSE(xn[i]), acc1); PIN(); }
        }
        if constexpr (n == 9) {
            sm.red1[w][lane] = acc1;
            arrive(sm.cnt[0], lane);
            PIN();
        }
        if constexpr (n == QN) {
            if (__builtin_expect(__builtin_amdgcn_readfirstlane(qok) == 0, 0)) {
#pragma nounroll
                for (unsigned s = 0; s < SPIN * 64; ++s) {
                    __builtin_amdgcn_s_sleep(1);
                    FRESH(); qok = sm.qf[lane]; FRESH(); qv = *qs; FRESH();
                    if (__builtin_amdgcn_readfirstlane(qok) != 0) break;
                }
            }
            qe = qv & lowq; qo = qv & highq;
        }
        if constexpr (n >= QN) {
            constexpr int NS = 25 - QN, lo = 8 * (n - QN) / NS, hi = 8 * (n + 1 - QN) / NS;
            v4f d[8];
#pragma unroll
            for (int p = 0; p < 8; ++p) if (p >= lo && p < hi) { d[p] = mfma(K[2 * p], qe, v4f{0.f, 0.f, 0.f, 0.f}); PIN(); }
#pragma unroll
            for (int p = 0; p < 8; ++p) if (p >= lo && p < hi) { d[p] = mfma(K[2 * p + 1], qo, d[p]); PIN(); }
#pragma unroll
            for (int p = 0; p < 8; ++p) if (p >= lo && p < hi) { ta[p] = d[p].x + dpp<X1>(d[p].y); tb_[p] = d[p].z + dpp<X1>(d[p].w); }
            PIN();
#pragma unroll
            for (int p = 0; p < 8; ++p) if (p >= lo && p < hi) sc[p] = ta[p] + dpp<X2>(tb_[p]);
            PIN();
        }
    });
    float m = fmaxf(fmaxf(fmaxf(sc[0], sc[1]), fmaxf(sc[2], sc[3])), fmaxf(fmaxf(sc[4], sc[5]), fmaxf(sc[6], sc[7])));
    m = dpp<0>(m);
    m = fmaxf(m, dpp<HMIR>(m));
    m = max32(max16(m)) * C2;
    unsigned pb[8];
    float l = 0.f;
#define EXPS(a, b) do { _Pragma("unroll") for (int i = a; i < b; ++i) { \
        const float p = __builtin_amdgcn_exp2f(dpp<0>(sc[i] * C2 - m)); pb[i] = cvtpk(p, p); l += p; } } while (0)
    PIN();

    v4f o = {0.f, 0.f, 0.f, 0.f};
    unsigned mo[4];
    int oml;
    auto vstep = [&](auto t_, auto plan_) __attribute__((always_inline)) {
        constexpr int t = decltype(t_)::v, plan = decltype(plan_)::v;
        v4u B;
#define MKB() do { const unsigned pn = pb[t >> 1], pf = dppu<HMIR>(pn); \
        B = (t & 1) ? v4u{pf & msk[0], pf & msk[1], pn & msk[2], pn & msk[3]} : v4u{pn & msk[0], pn & msk[1], pf & msk[2], pf & msk[3]}; } while (0)
        if constexpr (t > 0) MKB();
        LANDED(25 + t);
        if constexpr (t + WIN < 19) PACE(25 + t);
        PIN();
        if constexpr (t + WIN < 16) ISSUE(25 + t + WIN);
        else {
            constexpr int lo = t + WIN == 16 ? 0 : (plan >> (4 * (16 - t))) & 15, hi = (plan >> (4 * (15 - t))) & 15;
            steps<lo, hi>(STEP(p) { constexpr int p = decltype(p_)::v; if constexpr (plan >> 24) LDO(p); else LDD(p); });
        }
        if constexpr (t + WIN < 18) TICK();
        PIN();
        if constexpr (t == 0) { EXPS(0, 4); MKB(); }
        o = mfma(V[t], B, o);
        if constexpr (t == 1) {
            EXPS(4, 8);
            l += dpp<HMIR>(l);
            l = add32(add16(l));
            if ((lane & 55) == 0) sm.mw[r][w] = v2f{m, l};
            arrive(sm.cnt[1], lane);
        }
        if constexpr (t == 6) {
#pragma unroll
            for (int k = 0; k < 4; ++k) { mo[k] = ((lane >> 2) & 3) == k ? ((lane & 2) ? 0xffff0000u : 0x0000ffffu) : 0u; KEEPV(mo[k]); }
            oml = ML_OFF + (h * 2 + (w >> 2)) * 256 + (lane & 60) * 4;
            KEEPV(oml);
        }
        PIN();
    };
    steps<0, 16 - WIN>(STEP(t) { vstep(t_, Idx<0>{}); });

#define SPARSE3(pb) v4u{(pb) & mo[0], (pb) & mo[1], (pb) & mo[2], (pb) & mo[3]}
#define ASK(a, d, o) "buffer_load_dwordx4 v[" #a ":" #d "], %[p], %[rs], 0 offen offset:" #o " sc1\n"
#define TAGS(m, b, d) "v_cmp_eq_u32_e64 " m ", %[t0], v" #b "\n v_cmp_eq_u32_e64 %[m2], %[t1], v" #d "\n s_and_b64 " m ", " m ", %[m2]\n"
#define KEEP(x, y, a, c) "v_cndmask_b32_e32 %[" #x "], %[" #x "], v" #a ", vcc\n v_cndmask_b32_e32 %[" #y "], %[" #y "], v" #c ", vcc\n"
#define LEAVE "s_andn2_b64 exec, exec, vcc\n s_cbranch_scc0 2f\n"
#define ASK2 ASK(136, 139, 0) ASK(140, 143, 1024)
#define ENTER "s_mov_b32 %[c], %[spin]\n1:\n s_waitcnt vmcnt(0)\n"
#define ROUND "s_sub_u32 %[c], %[c], 1\n s_cbranch_scc0 1b\n2: s_mov_b64 exec, -1"
#define POLL_IN(p_) [p] "v"(p_), [rs] "s"(dw), [t0] "s"(T0), [t1] "s"(T1), [spin] "n"(SPIN)
#define POLL1(g_, p_) do { unsigned long long m2_; unsigned c_; \
        asm volatile(ASK(136, 139, 0) ENTER TAGS("vcc", 137, 139) KEEP(gx, gy, 136, 138) LEAVE ASK(136, 139, 0) ROUND \
                     : [gx] "+v"(g_.x), [gy] "+v"(g_.y), [m2] "=&s"(m2_), [c] "=&s"(c_), "=&{v[136:143]}"(xa) \
                     : POLL_IN(p_) : "vcc", "scc", "memory"); \
    } while (0)
#define POLL2(ga, gb, p_) do { unsigned long long m_, m2_; unsigned c_; \
        asm volatile(ENTER TAGS("%[m]", 137, 139) TAGS("vcc", 141, 143) "s_and_b64 vcc, vcc, %[m]\n" \
                     KEEP(gx, gy, 136, 138) KEEP(gz, gw, 140, 142) LEAVE ASK2 ROUND \
                     : [gx] "+v"(ga.x), [gy] "+v"(ga.y), [gz] "+v"(gb.x), [gw] "+v"(gb.y), [m] "=&s"(m_), [m2] "=&s"(m2_), [c] "=&s"(c_), "+{v[136:143]}"(xa) \
                     : POLL_IN(p_) : "vcc", "scc", "memory"); \
    } while (0)
#define DRAIN() asm volatile("s_waitcnt vmcnt(0)" ::"{v[136:143]}"(xa) : "memory")
    v8u xa;
    const v4u dw = {(unsigned)(size_t)a.ws, (unsigned)((size_t)a.ws >> 32) & 0xffffu, (unsigned)WS_BYTES, 0x00020000u};
    const float* const ts = (const float*)sm.red3[w] + ((lane >> 5) * 16 + 2 * j) * 4 + (r & 3);
    if (isO) {
        steps<16 - WIN, 16>(STEP(t) { vstep(t_, Idx<0x1000000 | OPLAN>{}); });
        sm.red2[w][lane] = o;
        BARRIER();
        PIN();
        steps<OPLAN & 15, 3>(STEP(p) { constexpr int p = decltype(p_)::v; LDO(p); });
        PIN();
        v4u ml = ld<SC1>(rw, oml);
        __builtin_amdgcn_s_sleep(PRE);
        if (ANY(BAD(ml))) {
#pragma nounroll
            for (unsigned s = 0; s < SPIN; ++s) {
                __builtin_amdgcn_s_sleep(QSL);
                FRESH();
                ml = ld<SC1>(rw, oml);
                if (!ANY(BAD(ml))) break;
            }
        }
        USE(S3[0]); USE(S3[1]); USE(S3[2]);
        PIN();
        asm volatile(ASK2 : "=&{v[136:143]}"(xa) : [p] "v"(og1), [rs] "s"(dw) : "memory");
        PIN();
        float M = fmaxf(asf(ml.x), dpp<ROR4>(asf(ml.x)));
        M = max32(max16(fmaxf(M, dpp<ROR8>(M))));
        const float e = __builtin_amdgcn_exp2f(asf(ml.x) - M);
        float L = e * asf(ml.z);
        L += dpp<ROR4>(L);
        L += dpp<ROR8>(L);
        const float wt = e * __builtin_amdgcn_rcpf(add32(add16(L)));
        const auto wz = __builtin_amdgcn_permlane32_swap(asu(wt), asu(wt), false, false);
        float wa = asf(wz[0]), wb = asf(wz[1]);
        v2u g[2];
        asm volatile("" : "=v"(g[0]), "=v"(g[1]), "+v"(wa), "+v"(wb));
        PIN();
        POLL2(g[0], g[1], og1);
        v4u B[4];
#pragma unroll
        for (int e = 0; e < 2; ++e) {
            const auto z = __builtin_amdgcn_permlane32_swap(g[e].x, g[e].y, false, false);
            float u = add16(lo16(z[0]) * wa + lo16(z[1]) * wb, hi16(z[0]) * wa + hi16(z[1]) * wb);
            u += dpp<ROR4>(u);
            u += dpp<ROR8>(u);
            const unsigned p = cvtpk(u, u);
            B[2 * e] = SPARSE3(dppu<0x44>(p)); B[2 * e + 1] = SPARSE3(dppu<0xEE>(p));
        }
        v4f acc[3] = {{0.f, 0.f, 0.f, 0.f}, {0.f, 0.f, 0.f, 0.f}, {0.f, 0.f, 0.f, 0.f}};
#pragma unroll
        for (int i = 0; i < 4; ++i)
#pragma unroll
            for (int c = 0; c < 3; ++c) {
                acc[c] = mfma(4 * c + i < 9 ? S1[4 * c + i] : S3[4 * c + i - 9], B[i], acc[c]);
                if (i == 3) sm.red3[c][w][lane] = acc[c];
            }
        BARRIER();
        if (w < 3) {
            const float y = tile_sum<132>(ts);
            put(rw, op2, asu(y), T0, asu(dpp<X1>(y)), T1);
        }
        DRAIN();
        __builtin_amdgcn_endpgm();
    } else {
        steps<16 - WIN, 16>(STEP(t) { vstep(t_, Idx<DPLAN>{}); });
        sm.red2[w][lane] = o;
        BARRIER();
        PIN();
        if (isD) {
            __builtin_amdgcn_s_sleep(DSL);
            PIN();
            steps<DPLAN & 15, 5>(STEP(p) { constexpr int p = decltype(p_)::v; LDD(p); });
            PIN();
            FRESH();
            float bv[5];
#pragma unroll
            for (int i = 0; i < 5; ++i) bv[i] = ((const float*)sm.hb)[36 * w + 8 * i + (r & 6) + (lane & 1)];
            bv[4] = r < 4 ? bv[4] : 0.f;
            v4f acc = {0.f, 0.f, 0.f, 0.f};
#pragma unroll
            for (int i = 0; i < 5; ++i) { const unsigned p = cvtpk(bv[i], bv[i]); acc = mfma(S3[i], SPARSE3(p), acc); }
            sm.red3[0][w][lane] = acc;
            BARRIER();
            if (w == 0) {
                const float y = tile_sum<132>(ts);
                put(rw, op2, asu(y), T0, asu(dpp<X1>(y)), T1);
            }
        } else if (sp < 3 && w < 3) {
            const int oc = cg * 8 + (lane & 1) * 4 + (lane & 2);
            float xf = lo16(__builtin_amdgcn_raw_buffer_load_b16(mkrs(a.x, D * 2), oc, 0, 0));
            v2u g;
            asm volatile("" : "=v"(g), "+v"(xf));
            __builtin_amdgcn_s_sleep(PRE2);
            POLL1(g, og2);
            float s = asf(g.x) + dpp<X2>(asf(g.y));
            s += dpp<ROR4>(s);
            s += dpp<ROR8>(s);
            s = rbf(add16(s));
            const auto z = __builtin_amdgcn_permlane32_swap(asu(s), asu(s), false, false);
            const float y0 = xf + rbf(asf(z[0]) * rbf(silu(s)));
            __builtin_amdgcn_raw_buffer_store_b16((unsigned short)cvtpk(y0, y0), mkrs(pout, D * 2), (lane & 60) == 32 ? oc : OOB, 0, SC1);
            if (cg == 0) __builtin_amdgcn_raw_buffer_store_b64(v2u{T0, T1}, rw, lane == 0 ? 0 : OOB, 0, SC1);
            DRAIN();
        }
        __builtin_amdgcn_s_waitcnt(0x0f70);
        __builtin_amdgcn_endpgm();
    }
}

extern "C" void kernel_launch(void* const* d_in, const int* in_sizes, int n_in,
                              void* d_out, int out_size, void* d_ws, size_t ws_size,
                              hipStream_t stream) {
    Args a;
    a.x = (const char*)d_in[0]; a.k = (const char*)d_in[1]; a.v = (const char*)d_in[2];
    a.wq = (const char*)d_in[3]; a.wo = (const char*)d_in[4];
    a.wg = (const char*)d_in[6]; a.wu = (const char*)d_in[7]; a.wd = (const char*)d_in[8];
    a.out = (char*)d_out; a.ws = (char*)d_ws;
    fused_decode<<<256, 576, 0, stream>>>(a);
}
```

```cpp
#include <hip/hip_runtime.h>
#include <hip/hip_bf16.h>
#include <stdint.h>

typedef unsigned v4u __attribute__((ext_vector_type(4)));
typedef unsigned v8u __attribute__((ext_vector_type(8)));
typedef unsigned v2u __attribute__((ext_vector_type(2)));
typedef float v4f __attribute__((ext_vector_type(4)));
typedef float v2f __attribute__((ext_vector_type(2)));
typedef __bf16 bf16x2 __attribute__((ext_vector_type(2)));
typedef __bf16 bf16x8 __attribute__((ext_vector_type(8)));
typedef __amdgpu_buffer_rsrc_t rsrc_t;
#define LAS __attribute__((address_space(3)))

constexpr int WIN = 4;
constexpr int XP = 7, QCHK = 18;
constexpr int R0 = 10, RB = 31, TA = 134;
constexpr int N1 = 5, TB1 = 315, TB10 = 275;
constexpr int NADJ = 3, TL0 = 136;
constexpr int TX = 160, TQ = 571, TH = 859;
constexpr int QSL = 8, PRE = 8, PRE2 = 50, DSL = 4;
constexpr int OPLAN = 0x1233, DPLAN = 0x1234;

constexpr int D = 576, F = 4608;
constexpr int G1_OFF = 4096;
constexpr int ML_OFF = 57344;
constexpr int P1_OFF = 65536;
constexpr int P2_OFF = P1_OFF + 262144;
constexpr int WS_BYTES = P2_OFF + 144 * 1024;
constexpr int OOB = 0x7ffffff0;
constexpr int NT = 2, SC1 = 16;
constexpr unsigned SPIN = 1u << 16;
constexpr float LOG2E = 1.4426950408889634f;
constexpr float C2 = 0.088388347648318440f * LOG2E;

__host__ __device__ constexpr int map_h(int b) { return (b & 7) + 8 * (b >> 7); }
__host__ __device__ constexpr int map_sp(int b) { return (b >> 3) & 15; }

struct Args {
    const char *x, *wq, *wg, *wu, *k, *v, *wo, *wd;
    char *ws, *out;
};

__device__ __forceinline__ float asf(unsigned u) { return __uint_as_float(u); }
__device__ __forceinline__ unsigned asu(float f) { return __float_as_uint(f); }
__device__ __forceinline__ float lo16(unsigned u) { return asf(u << 16); }
__device__ __forceinline__ float hi16(unsigned u) { return asf(u & 0xffff0000u); }
__device__ __forceinline__ unsigned cvtpk(float lo, float hi) {
    return __builtin_bit_cast(unsigned, __builtin_convertvector(v2f{lo, hi}, bf16x2));
}
__device__ __forceinline__ float rbf(float x) { return lo16(cvtpk(x, x)); }
__device__ __forceinline__ v4f mfma(v4u a, v4u b, v4f c) {
    return __builtin_amdgcn_mfma_f32_16x16x32_bf16(__builtin_bit_cast(bf16x8, a), __builtin_bit_cast(bf16x8, b), c, 0, 0, 0);
}
template <int C> __device__ __forceinline__ float dpp(float v) { return asf(__builtin_amdgcn_mov_dpp(asu(v), C, 0xf, 0xf, true)); }
template <int C> __device__ __forceinline__ unsigned dppu(unsigned v) { return __builtin_amdgcn_mov_dpp(v, C, 0xf, 0xf, true); }
constexpr int X1 = 0xB1, X2 = 0x4E, HMIR = 0x141, MIR = 0x140, ROR4 = 0x124, ROR8 = 0x128;
__device__ __forceinline__ float add16(float a, float b) { auto r = __builtin_amdgcn_permlane16_swap(asu(a), asu(b), false, false); return asf(r[0]) + asf(r[1]); }
__device__ __forceinline__ float add16(float v) { return add16(v, v); }
__device__ __forceinline__ float add32(float v) { auto r = __builtin_amdgcn_permlane32_swap(asu(v), asu(v), false, false); return asf(r[0]) + asf(r[1]); }
__device__ __forceinline__ float max16(float v) { auto r = __builtin_amdgcn_permlane16_swap(asu(v), asu(v), false, false); return fmaxf(asf(r[0]), asf(r[1])); }
__device__ __forceinline__ float max32(float v) { auto r = __builtin_amdgcn_permlane32_swap(asu(v), asu(v), false, false); return fmaxf(asf(r[0]), asf(r[1])); }
__device__ __forceinline__ float sum8(float v) { v += dpp<X1>(v); v += dpp<X2>(v); v += dpp<HMIR>(v); return v; }
__device__ __forceinline__ float max8(float v) { v = fmaxf(v, dpp<X1>(v)); v = fmaxf(v, dpp<X2>(v)); return fmaxf(v, dpp<HMIR>(v)); }
__device__ __forceinline__ float sum64(float v) { v = sum8(v); v += dpp<MIR>(v); return add32(add16(v)); }
__device__ __forceinline__ float silu(float g) { return g * __builtin_amdgcn_rcpf(1.f + __builtin_amdgcn_exp2f(-LOG2E * g)); }
__device__ __forceinline__ rsrc_t mkrs(const void* p, int bytes) { return __builtin_amdgcn_make_buffer_rsrc((void*)p, 0, bytes, 0x00020000); }
template <int AUX> __device__ __forceinline__ v4u ld(rsrc_t r, int vo, int so = 0) { return __builtin_amdgcn_raw_buffer_load_b128(r, vo, so, AUX); }
__device__ __forceinline__ void put(rsrc_t r, int vo, unsigned a, unsigned t0, unsigned b, unsigned t1) {
    __builtin_amdgcn_raw_buffer_store_b128(v4u{a, t0, b, t1}, r, vo, 0, SC1);
}
template <typename T> __device__ __forceinline__ T sel(bool c, T a, T b) { return c ? a : b; }
template <int I> struct Idx { static constexpr int v = I; };
template <int I, int N, typename Fn> __device__ __forceinline__ void steps(Fn&& f) {
    if constexpr (I < N) { f(Idx<I>{}); steps<I + 1, N>(f); }
}
#define STEP(n) [&](auto n##_) __attribute__((always_inline))
#define USE(x) asm volatile("" ::"v"(x))
#define KEEPV(x) asm volatile("" : "+v"(x))
#define KEEPS(x) asm volatile("" : "+s"(x))
#define FRESH() asm volatile("" ::: "memory")
#define PIN() __builtin_amdgcn_sched_barrier(0)
#define BAD(g) (((g).y ^ T0) | ((g).w ^ T1))
#define ANY(x) __builtin_expect(__builtin_amdgcn_ballot_w64((x) != 0u) != 0ull, 0)

struct Smem {
    v4f red1[8][64], red2[8][64], red3[3][8][64];
    v4u xn[144];
    v4f hb[80];
    unsigned cnt[2][64];
    unsigned qf[64], xf[64];
    v2u q[64];
    v2f mw[2][8];
    float fw[2][8];
    v2u tag;
};

__device__ __forceinline__ void arrive(unsigned* c, int lane) {
    FRESH();
    __hip_atomic_fetch_add(c + lane, 1u, __ATOMIC_RELAXED, __HIP_MEMORY_SCOPE_WORKGROUP);
    FRESH();
}
__device__ __forceinline__ void await8(const unsigned* c, int lane) {
#pragma nounroll
    for (unsigned s = 0; s < SPIN * 64; ++s) {
        FRESH();
        const unsigned n = c[lane];
        FRESH();
        if (__builtin_amdgcn_readfirstlane(n) == 8) break;
        __builtin_amdgcn_s_sleep(1);
    }
}
#define BARRIER() asm volatile("s_waitcnt lgkmcnt(0)\n s_barrier" ::: "memory")
template <int STEP2> __device__ __forceinline__ float tile_sum(const float* p) {
    float a = 0.f, b = 0.f;
#pragma unroll
    for (int w = 0; w < 8; ++w) { a += p[w * 256]; b += p[w * 256 + STEP2]; }
    return a + b;
}
__device__ __forceinline__ unsigned now() { return (unsigned)__builtin_amdgcn_s_memrealtime(); }
__device__ __forceinline__ void until(unsigned t0, unsigned dt) {
#pragma nounroll
    for (unsigned i = 0; i < SPIN; ++i) {
        if (now() - t0 >= dt) break;
        __builtin_amdgcn_s_sleep(2);
    }
}

__global__ void __launch_bounds__(576, 3) __attribute__((aligned(4096))) fused_decode(Args a) {
    __shared__ Smem sm;
    asm volatile("" ::: "v160");
    unsigned long long t64 = __builtin_amdgcn_s_memrealtime();
    const int tid = threadIdx.x, lane = tid & 63, w = __builtin_amdgcn_readfirstlane(tid >> 6), b = blockIdx.x;
    const int h = map_h(b), sp = map_sp(b);
    const int r = lane >> 3, j = lane & 7;
    const bool isQ = sp < 4, has1 = sp <= 12, isO = !has1, isD = has1 && !isQ;
    const int gi = 9 * h + sp - 4;

    unsigned zc0 = 0, zc1 = 0;
    {
        unsigned long long pc; unsigned n;
        asm volatile("s_cmp_lg_u32 %5, 8\n s_cbranch_scc1 1f\n s_getpc_b64 %2\n.Lcode_here:\n s_mov_b32 %3, .Lcode_last-.Lcode_here\n"
                     "global_load_dword %0, %4, %2\n v_or_b32 %1, 0x2000, %4\n v_min_u32 %1, %3, %1\n global_load_dword %1, %1, %2\n1:"
                     : "+v"(zc0), "+v"(zc1), "=&s"(pc), "=&s"(n) : "v"(lane * 128), "s"(w) : "scc");
    }
    if (w == 8) {
        const rsrc_t rx = mkrs(a.x, D * 2), rw = mkrs(a.ws, WS_BYTES);
        unsigned zt = 0;
        if (isO) {
            const char* t = sel(sp == 13, lane < 2 ? a.k : a.wq, sel(sp == 14, lane < 2 ? a.v : sel(h < 8, a.wg, a.wu), lane < 2 ? a.wd : a.out));
            const unsigned row = sel(sp == 13, lane == 2 ? 256u * 8192u : 575u * 8192u, sel(sp == 14, (lane == 2 ? 228u : lane == 3 ? 456u : 575u) * (F * 2), 0u));
            const unsigned o = lane >= 2 ? row + (b & 7) * 128u : sel(sp == 15, (288u * h + 287u * lane) * (D * 2), ((unsigned)h << 21) + lane * 0x1fff80u);
            if (lane < sel(sp == 13, 4, sel(sp == 14, 5, 3))) zt = __builtin_nontemporal_load((const __attribute__((address_space(1))) unsigned*)(t + o));
            PIN();
        }
        KEEPS(t64);
        const unsigned tb = (unsigned)t64;
        sm.cnt[0][lane] = 0; sm.cnt[1][lane] = 0; sm.qf[lane] = 0; sm.xf[lane] = 0;
        BARRIER();
        PIN();
        until(tb, TX);
        PIN();
        const v4u xa = ld<0>(rx, lane * 16);
        const unsigned xb = __builtin_amdgcn_raw_buffer_load_b32(rx, 1024 + lane * 4, 0, 0);
        const v2u ep = __builtin_amdgcn_raw_buffer_load_b64(rw, 0, 0, SC1);
        PIN();
        const unsigned z = __builtin_amdgcn_raw_buffer_load_b32(rw, lane < 15 ? G1_OFF + (b * 15 + lane) * 128 : OOB, 0, SC1);
        PIN();

        const float e[10] = {lo16(xa.x), hi16(xa.x), lo16(xa.y), hi16(xa.y), lo16(xa.z), hi16(xa.z), lo16(xa.w), hi16(xa.w), lo16(xb), hi16(xb)};
        float s = 0.f;
#pragma unroll
        for (int i = 0; i < 10; ++i) s += e[i];
        const float mean = sum64(s) * (1.f / D);
        float c[10];
#pragma unroll
        for (int i = 0; i < 10; ++i) c[i] = e[i] - mean;
        float q = c[8] * c[8] + c[9] * c[9];
        q = lane < 32 ? q : 0.f;
#pragma unroll
        for (int i = 0; i < 8; ++i) q += c[i] * c[i];
        const float rs = __builtin_amdgcn_rsqf(sum64(q) * (1.f / D) + 1e-5f);
        unsigned n[10];
#pragma unroll
        for (int i = 0; i < 10; ++i) { const float t = c[i] * rs; n[i] = cvtpk(t, t); }
        sm.xn[2 * lane] = v4u{n[0], n[1], n[2], n[3]};
        sm.xn[2 * lane + 1] = v4u{n[4], n[5], n[6], n[7]};
        if (lane < 32) ((v2u*)&sm.xn[128])[lane] = v2u{n[8], n[9]};
        const unsigned T0 = __builtin_amdgcn_readfirstlane((ep.x + 0x9E3779B1u) | 1u), T1 = __builtin_amdgcn_readfirstlane(ep.y + 0x85EBCA77u);
        if (lane == 0) sm.tag = v2u{T0, T1};
        FRESH();
        sm.xf[lane] = 1;
        FRESH();

        USE(z); USE(zt);
        asm volatile("s_waitcnt vmcnt(0)" : "+v"(zc0), "+v"(zc1));

        v4u g;
        const int oq = G1_OFF + (64 * h + lane) * 16;
        bool asked = false;
        if (has1) {
#pragma nounroll
            for (unsigned i = 0; i < SPIN * 64; ++i) {
                FRESH();
                const unsigned n = sm.cnt[0][lane];
                FRESH();
                if (__builtin_amdgcn_readfirstlane(n) == 8) break;
                if (isD && !asked && now() - tb >= TQ) { g = ld<SC1>(rw, oq); asked = true; }
                __builtin_amdgcn_s_sleep(1);
            }
            const float y = tile_sum<160>((const float*)sm.red1 + ((lane >> 5) * 16 + j) * 4 + (r & 3));
            const unsigned pk = cvtpk(y, dpp<X1>(y));
            put(rw, (lane & 3) == 0 ? G1_OFF + (sel(isQ, 256 * h + 64 * sp, 4096 + gi * 64) + lane) * 4 : OOB, pk, T0, dppu<0xAA>(pk), T1);
        }
        if (!asked) { until(tb, TQ); g = ld<SC1>(rw, oq); }
#pragma nounroll
        for (unsigned i = 0; i < SPIN; ++i) {
            if (!ANY(BAD(g))) break;
            __builtin_amdgcn_s_sleep(QSL);
            FRESH();
            g = ld<SC1>(rw, oq);
        }
        sm.q[lane] = v2u{g.x, g.z};
        FRESH();
        sm.qf[lane] = 1;
        FRESH();

        await8(sm.cnt[1], lane);
        {
            const v2f mz = sm.mw[r & 1][j];
            const float M = max8(mz.x), f = __builtin_amdgcn_exp2f(mz.x - M);
            put(rw, (lane & 55) == 0 ? ML_OFF + ((h * 2 + r) * 16 + sp) * 16 : OOB, asu(M), T0, asu(sum8(f * mz.y)), T1);
            if (lane < 16) sm.fw[r][j] = f;
        }
        if (isD) {
            const int o0 = G1_OFF + (1024 + 72 * h + lane) * 16, o1 = lane < 8 ? o0 + 1024 : OOB;
            const unsigned lm = lane < 8 ? ~0u : 0u;
            v4u g0, u0, g1, u1;
            until(tb, TH);
#pragma nounroll
            for (unsigned i = 0; i < SPIN; ++i) {
                FRESH();
                g0 = ld<SC1>(rw, o0); u0 = ld<SC1>(rw, o0, (F / 4) * 16);
                g1 = ld<SC1>(rw, o1); u1 = ld<SC1>(rw, o1, (F / 4) * 16);
                if (!ANY(BAD(g0) | BAD(u0) | ((BAD(g1) | BAD(u1)) & lm))) break;
                __builtin_amdgcn_s_sleep(QSL);
            }
#define HID(g, u) v4f{rbf(silu(lo16(g.x))) * lo16(u.x), rbf(silu(hi16(g.x))) * hi16(u.x), rbf(silu(lo16(g.z))) * lo16(u.z), rbf(silu(hi16(g.z))) * hi16(u.z)}
            sm.hb[lane] = HID(g0, u0);
            if (lane < 16) sm.hb[64 + lane] = HID(g1, u1);
        }
        FRESH();
        const v4f fa = *(const v4f*)&sm.fw[r & 1][0], fb = *(const v4f*)&sm.fw[r & 1][4];
        int op1 = P1_OFF + ((h * 2 + (r & 1)) * 8 + (r & 6) + (j >> 2)) * 1024 + (sp * 4 + (j & 3)) * 16;
        KEEPV(op1);
        USE(fa); USE(fb);
        __builtin_amdgcn_s_setprio(3);
        BARRIER();
        v4f p[8];
#pragma unroll
        for (int i = 0; i < 8; ++i) p[i] = sm.red2[i][lane];
        PIN();
        v4f o = fa.x * p[0] + fa.y * p[1] + fa.z * p[2] + fa.w * p[3];
        o += fb.x * p[4] + fb.y * p[5] + fb.z * p[6] + fb.w * p[7];
        put(rw, op1, cvtpk(o.x, o.y), T0, cvtpk(o.z, o.w), T1);
        asm volatile(".Lcode_last:");
        __builtin_amdgcn_endpgm();
    }

    const bool isU = gi >= 72;
    const int c1 = sel(isQ, 4 * h + sp, sel(isU, gi - 72, gi));
    const int ld1 = sel(isQ, 8192, F * 2);
    const int kvb = w * 1024 + ((sp & 1) << 14 | (sp >> 1) << 18);
#define KST(t) (((t) & 1) << 13 | ((t) >> 1) << 15)
    v4u S1[9], K[16], V[16], S3[5];
#define LD1(i) S1[i] = (i) < 8 ? ld<NT>(r1, o1, ((i) & 3) * sa1 + ((i) >> 2) * sb1) : ld<NT>(r1, o1t)
#define LDK(t) K[t] = ld<NT>(rk, ok_, KST(t))
#define LDV(t) V[t] = ld<NT>(rv, ov, KST(t))
#define LDO(n) S3[n] = ld<NT>(r3, o3, ((n) + 1) * 2 * D * 2)
#define LDD(n) S3[n] = ld<NT>(r3, (n) < 4 ? o3 : o3x, (n) * 8 * D * 2)
#define ISSUE(p) do { if constexpr ((p) < 9) LD1(p); else if constexpr ((p) < 25) LDK((p) - 9); else LDV((p) - 25); } while (0)
#define LANDED(p) do { if constexpr ((p) < 9) USE(S1[p]); else if constexpr ((p) < 25) USE(K[(p) - 9]); else USE(V[(p) - 25]); } while (0)
#define WAITT(t, base) do { unsigned v_; \
        asm volatile("1: s_memrealtime vcc\n s_waitcnt lgkmcnt(0)\n s_sub_u32 %0, vcc_lo, %2\n s_cmp_lt_i32 %0, %1\n s_cbranch_scc1 1b" \
                     : "=&s"(v_) : "n"(t), "s"(base) : "scc", "vcc"); } while (0)
    const int orow = (32 * w + 8 * (r >> 1) + (r & 1)) * (D * 2), offo = 256 * h * (D * 2) + (sp - 13) * 384;
    int o1 = sel(has1, (8 * w + r) * ld1, orow) + j * 16, o1t = sel(has1, (512 + 8 * w + r) * ld1, orow + 256) + j * 16;
    int off1 = sel(has1, c1 * 128, offo), len1 = sel(has1, (D - 1) * ld1 + 128, 255 * D * 2 + 384);
    int pick1 = sel(isQ, 0, sel(isO, 3, sel(isU, 2, 1)));
    const int sa1 = sel(has1, 64 * ld1, 2 * D * 2), sb1 = sel(has1, 256 * ld1, 128);
    const char *pq = a.wq, *pg = a.wg, *pu = a.wu, *po = a.wo;
    asm volatile("" : "+v"(o1));
    asm volatile("" : "+s"(off1), "+s"(len1), "+s"(pick1));
    asm volatile("" : "+s"(pq), "+s"(pg), "+s"(pu), "+s"(po));
    const char* const p1 = sel(pick1 < 2, sel(pick1 == 0, pq, pg), sel(pick1 == 2, pu, po)) + (unsigned)off1;
    const rsrc_t r1 = mkrs(p1, len1);
    LD1(0);
    PIN();
    KEEPS(t64);
    unsigned tb = (unsigned)t64, tn = tb;
    int adj;
    asm volatile("s_and_b32 %0, %1, 0x380\n s_cmp_lg_u32 %0, %2\n s_cselect_b32 %0, -1, 0" : "=&s"(adj) : "s"((unsigned)(size_t)p1), "s"(sel(has1, 0x180, 1)) : "scc");
    PIN();

#define SCHED(n) (TA + ((n) <= N1 ? TB1 * (n) : TB1 * N1 + TB10 * ((n) - N1)) / 10)
#define PACE(n) do { unsigned v_; \
        asm volatile("s_cmp_ge_i32 %1, %2\n s_cbranch_scc1 2f\n1: s_memrealtime vcc\n s_waitcnt lgkmcnt(0)\n" \
                     "s_sub_u32 %0, vcc_lo, %3\n s_cmp_lt_i32 %0, %2\n s_cbranch_scc1 1b\n2:" \
                     : "=&s"(v_) : "s"(tn - tb), "n"(SCHED(n)), "s"(tb) : "scc", "vcc"); } while (0)
#define TICK() do { PIN(); tn = now(); } while (0)
#define ADJUST(n) do { unsigned d_; TICK(); \
        asm volatile("s_sub_u32 %1, %2, %0\n s_sub_u32 %1, %1, %3\n s_max_i32 %1, %1, 0\n s_and_b32 %1, %1, %4\n s_add_u32 %0, %0, %1" \
                     : "+s"(tb), "=&s"(d_) : "s"(tn), "n"(TL0 + (n) * RB), "s"(adj) : "scc"); } while (0)

    const rsrc_t rw = mkrs(a.ws, WS_BYTES);
    const rsrc_t rk = mkrs(a.k + ((unsigned)h << 21), 2 << 20);
    int ok_ = kvb + ((lane >> 2) & 3) * 256 + (lane >> 4) * 64 + (lane & 3) * 16;
    int off3 = sel(isD, 288 * h * (D * 2) + (sp - 4) * 128, offo);
    int len3 = sel(isO, 255 * D * 2 + 384, sel(isD, 287 * D * 2 + 128, 0));
    int o3 = sel(isD, (36 * w + r) * (D * 2) + j * 16, o1t);
    int o3x = r < 4 ? o3 : OOB;
    const int cg = 9 * h + 3 * sp + w;
    int ov = kvb + lane * 16;
    int og1 = P1_OFF + (h * 8 + w) * 2048 + lane * 16;
    const int pr = sel(isD, 16 + h, h);
    int op2 = ((lane ^ pr) & 1) != 0 ? OOB : P2_OFF + (sel(isD, sp - 4, 3 * sp - 39 + w) * 16 + (lane >> 2)) * 1024 + pr * 32 + (lane & 2) * 8;
    int og2 = P2_OFF + cg * 1024 + lane * 16;
    unsigned lowq = (lane & 4) ? 0u : ~0u, highq = ~lowq;
    unsigned msk[4];
#pragma unroll
    for (int k = 0; k < 4; ++k) { msk[k] = (j >> 1) == k ? ((lane & 1) ? 0xffff0000u : 0x0000ffffu) : 0u; KEEPV(msk[k]); }
#define SPARSE(pb) v4u{(pb) & msk[0], (pb) & msk[1], (pb) & msk[2], (pb) & msk[3]}
    KEEPS(off3); KEEPS(len3);
    KEEPV(o1t); KEEPV(ok_); KEEPV(o3); KEEPV(o3x); KEEPV(ov); KEEPV(og1); KEEPV(op2); KEEPV(og2); KEEPV(lowq); KEEPV(highq);
    PIN();
    steps<1, WIN>(STEP(p) { constexpr int p = decltype(p_)::v; WAITT(R0 + p * RB, tb); PIN(); LD1(p); PIN(); });
    const char *pv = a.v, *pd = a.wd, *pout = a.out;
    asm volatile("" : "+s"(pv), "+s"(pd));
    const char* p3 = sel(isD, pd, po) + (unsigned)off3;
    KEEPS(p3);
    const rsrc_t rv = mkrs(pv + ((unsigned)h << 21), 2 << 20);
    const rsrc_t r3 = mkrs(p3, len3);
    PIN();
    BARRIER();
    PIN();

    static_assert(WIN >= 4 && WIN <= 6 && XP < 9 && QCHK - WIN + 9 > 9, "schedule");
    const v4u* const qs = (const v4u*)sm.q + (r & 1) * 16 + (lane >> 4) * 4 + (lane & 3);
    v4u qv, qe, qo;
    v2u tg;
    unsigned xn[9], xok, qok, T0, T1;
    float sc[8], ta[8], tb_[8];
    v4f acc1 = {0.f, 0.f, 0.f, 0.f};
#define LDX() do { FRESH(); xok = sm.xf[lane]; FRESH(); \
        _Pragma("unroll") for (int i = 0; i < 9; ++i) xn[i] = ((const unsigned*)sm.xn)[64 * i + 8 * w + r]; \
        tg = sm.tag; FRESH(); } while (0)
    steps<0, 25>(STEP(n) {
        constexpr int n = decltype(n_)::v, QN = QCHK - WIN + 9;
        if constexpr (n == XP) LDX();
        if constexpr (n == QN) { FRESH(); qok = sm.qf[lane]; FRESH(); qv = *qs; FRESH(); }
        LANDED(n);
        if constexpr (n < NADJ) ADJUST(n);
        PACE(n);
        PIN();
        ISSUE(n + WIN);
        TICK();
        PIN();
        if constexpr (n == XP) {
            if (__builtin_expect(__builtin_amdgcn_readfirstlane(xok) == 0, 0)) {
#pragma nounroll
                for (unsigned s = 0; s < SPIN * 64; ++s) {
                    __builtin_amdgcn_s_sleep(1);
                    LDX();
                    if (__builtin_amdgcn_readfirstlane(xok) != 0) break;
                }
            }
            T0 = __builtin_amdgcn_readfirstlane(tg.x); T1 = __builtin_amdgcn_readfirstlane(tg.y);
            PIN();
        }
        if constexpr (n >= XP && n < 9) {
            constexpr int lo = n == XP ? 0 : n, hi = n + 1;
#pragma unroll
            for (int i = 0; i < 9; ++i) if (i >= lo && i < hi) { acc1 = mfma(S1[i], SPARSE(xn[i]), acc1); PIN(); }
        }
        if constexpr (n == 9) {
            sm.red1[w][lane] = acc1;
            arrive(sm.cnt[0], lane);
            PIN();
        }
        if constexpr (n == QN) {
            if (__builtin_expect(__builtin_amdgcn_readfirstlane(qok) == 0, 0)) {
#pragma nounroll
                for (unsigned s = 0; s < SPIN * 64; ++s) {
                    __builtin_amdgcn_s_sleep(1);
                    FRESH(); qok = sm.qf[lane]; FRESH(); qv = *qs; FRESH();
                    if (__builtin_amdgcn_readfirstlane(qok) != 0) break;
                }
            }
            qe = qv & lowq; qo = qv & highq;
        }
        if constexpr (n >= QN) {
            constexpr int NS = 25 - QN, lo = 8 * (n - QN) / NS, hi = 8 * (n + 1 - QN) / NS;
            v4f d[8];
#pragma unroll
            for (int p = 0; p < 8; ++p) if (p >= lo && p < hi) { d[p] = mfma(K[2 * p], qe, v4f{0.f, 0.f, 0.f, 0.f}); PIN(); }
#pragma unroll
            for (int p = 0; p < 8; ++p) if (p >= lo && p < hi) { d[p] = mfma(K[2 * p + 1], qo, d[p]); PIN(); }
#pragma unroll
            for (int p = 0; p < 8; ++p) if (p >= lo && p < hi) { ta[p] = d[p].x + dpp<X1>(d[p].y); tb_[p] = d[p].z + dpp<X1>(d[p].w); }
            PIN();
#pragma unroll
            for (int p = 0; p < 8; ++p) if (p >= lo && p < hi) sc[p] = ta[p] + dpp<X2>(tb_[p]);
            PIN();
        }
    });
    float m = fmaxf(fmaxf(fmaxf(sc[0], sc[1]), fmaxf(sc[2], sc[3])), fmaxf(fmaxf(sc[4], sc[5]), fmaxf(sc[6], sc[7])));
    m = dpp<0>(m);
    m = fmaxf(m, dpp<HMIR>(m));
    m = max32(max16(m)) * C2;
    unsigned pb[8];
    float l = 0.f;
#define EXPS(a, b) do { _Pragma("unroll") for (int i = a; i < b; ++i) { \
        const float p = __builtin_amdgcn_exp2f(dpp<0>(sc[i] * C2 - m)); pb[i] = cvtpk(p, p); l += p; } } while (0)
    PIN();

    v4f o = {0.f, 0.f, 0.f, 0.f};
    unsigned mo[4];
    int oml;
    auto vstep = [&](auto t_, auto plan_) __attribute__((always_inline)) {
        constexpr int t = decltype(t_)::v, plan = decltype(plan_)::v;
        v4u B;
#define MKB() do { const unsigned pn = pb[t >> 1], pf = dppu<HMIR>(pn); \
        B = (t & 1) ? v4u{pf & msk[0], pf & msk[1], pn & msk[2], pn & msk[3]} : v4u{pn & msk[0], pn & msk[1], pf & msk[2], pf & msk[3]}; } while (0)
        if constexpr (t > 0) MKB();
        LANDED(25 + t);
        if constexpr (t + WIN < 19) PACE(25 + t);
        PIN();
        if constexpr (t + WIN < 16) ISSUE(25 + t + WIN);
        else {
            constexpr int lo = t + WIN == 16 ? 0 : (plan >> (4 * (16 - t))) & 15, hi = (plan >> (4 * (15 - t))) & 15;
            steps<lo, hi>(STEP(p) { constexpr int p = decltype(p_)::v; if constexpr (plan >> 24) LDO(p); else LDD(p); });
        }
        if constexpr (t + WIN < 18) TICK();
        PIN();
        if constexpr (t == 0) { EXPS(0, 4); MKB(); }
        o = mfma(V[t], B, o);
        if constexpr (t == 1) {
            EXPS(4, 8);
            l += dpp<HMIR>(l);
            l = add32(add16(l));
            if ((lane & 55) == 0) sm.mw[r][w] = v2f{m, l};
            arrive(sm.cnt[1], lane);
        }
        if constexpr (t == 6) {
#pragma unroll
            for (int k = 0; k < 4; ++k) { mo[k] = ((lane >> 2) & 3) == k ? ((lane & 2) ? 0xffff0000u : 0x0000ffffu) : 0u; KEEPV(mo[k]); }
            oml = ML_OFF + (h * 2 + (w >> 2)) * 256 + (lane & 60) * 4;
            KEEPV(oml);
        }
        PIN();
    };
    steps<0, 16 - WIN>(STEP(t) { vstep(t_, Idx<0>{}); });

#define SPARSE3(pb) v4u{(pb) & mo[0], (pb) & mo[1], (pb) & mo[2], (pb) & mo[3]}
#define ASK(a, d, o) "buffer_load_dwordx4 v[" #a ":" #d "], %[p], %[rs], 0 offen offset:" #o " sc1\n"
#define TAGS(m, b, d) "v_cmp_eq_u32_e64 " m ", %[t0], v" #b "\n v_cmp_eq_u32_e64 %[m2], %[t1], v" #d "\n s_and_b64 " m ", " m ", %[m2]\n"
#define KEEP(x, y, a, c) "v_cndmask_b32_e32 %[" #x "], %[" #x "], v" #a ", vcc\n v_cndmask_b32_e32 %[" #y "], %[" #y "], v" #c ", vcc\n"
#define LEAVE "s_andn2_b64 exec, exec, vcc\n s_cbranch_scc0 2f\n"
#define ASK2 ASK(136, 139, 0) ASK(140, 143, 1024)
#define ENTER "s_mov_b32 %[c], %[spin]\n1:\n s_waitcnt vmcnt(0)\n"
#define ROUND "s_sub_u32 %[c], %[c], 1\n s_cbranch_scc0 1b\n2: s_mov_b64 exec, -1"
#define POLL_IN(p_) [p] "v"(p_), [rs] "s"(dw), [t0] "s"(T0), [t1] "s"(T1), [spin] "n"(SPIN)
#define POLL1(g_, p_) do { unsigned long long m2_; unsigned c_; \
        asm volatile(ASK(136, 139, 0) ENTER TAGS("vcc", 137, 139) KEEP(gx, gy, 136, 138) LEAVE ASK(136, 139, 0) ROUND \
                     : [gx] "+v"(g_.x), [gy] "+v"(g_.y), [m2] "=&s"(m2_), [c] "=&s"(c_), "=&{v[136:143]}"(xa) \
                     : POLL_IN(p_) : "vcc", "scc", "memory"); \
    } while (0)
#define POLL2(ga, gb, p_) do { unsigned long long m_, m2_; unsigned c_; \
        asm volatile(ENTER TAGS("%[m]", 137, 139) TAGS("vcc", 141, 143) "s_and_b64 vcc, vcc, %[m]\n" \
                     KEEP(gx, gy, 136, 138) KEEP(gz, gw, 140, 142) LEAVE ASK2 ROUND \
                     : [gx] "+v"(ga.x), [gy] "+v"(ga.y), [gz] "+v"(gb.x), [gw] "+v"(gb.y), [m] "=&s"(m_), [m2] "=&s"(m2_), [c] "=&s"(c_), "+{v[136:143]}"(xa) \
                     : POLL_IN(p_) : "vcc", "scc", "memory"); \
    } while (0)
#define DRAIN() asm volatile("s_waitcnt vmcnt(0)" ::"{v[136:143]}"(xa) : "memory")
    v8u xa;
    const v4u dw = {(unsigned)(size_t)a.ws, (unsigned)((size_t)a.ws >> 32) & 0xffffu, (unsigned)WS_BYTES, 0x00020000u};
    const float* const ts = (const float*)sm.red3[w] + ((lane >> 5) * 16 + 2 * j) * 4 + (r & 3);
    if (isO) {
        steps<16 - WIN, 16>(STEP(t) { vstep(t_, Idx<0x1000000 | OPLAN>{}); });
        sm.red2[w][lane] = o;
        BARRIER();
        PIN();
        steps<OPLAN & 15, 3>(STEP(p) { constexpr int p = decltype(p_)::v; LDO(p); });
        PIN();
        v4u ml = ld<SC1>(rw, oml);
        __builtin_amdgcn_s_sleep(PRE);
        if (ANY(BAD(ml))) {
#pragma nounroll
            for (unsigned s = 0; s < SPIN; ++s) {
                __builtin_amdgcn_s_sleep(QSL);
                FRESH();
                ml = ld<SC1>(rw, oml);
                if (!ANY(BAD(ml))) break;
            }
        }
        USE(S3[0]); USE(S3[1]); USE(S3[2]);
        PIN();
        asm volatile(ASK2 : "=&{v[136:143]}"(xa) : [p] "v"(og1), [rs] "s"(dw) : "memory");
        PIN();
        float M = fmaxf(asf(ml.x), dpp<ROR4>(asf(ml.x)));
        M = max32(max16(fmaxf(M, dpp<ROR8>(M))));
        const float e = __builtin_amdgcn_exp2f(asf(ml.x) - M);
        float L = e * asf(ml.z);
        L += dpp<ROR4>(L);
        L += dpp<ROR8>(L);
        const float wt = e * __builtin_amdgcn_rcpf(add32(add16(L)));
        const auto wz = __builtin_amdgcn_permlane32_swap(asu(wt), asu(wt), false, false);
        float wa = asf(wz[0]), wb = asf(wz[1]);
        v2u g[2];
        asm volatile("" : "=v"(g[0]), "=v"(g[1]), "+v"(wa), "+v"(wb));
        PIN();
        POLL2(g[0], g[1], og1);
        v4u B[4];
#pragma unroll
        for (int e = 0; e < 2; ++e) {
            const auto z = __builtin_amdgcn_permlane32_swap(g[e].x, g[e].y, false, false);
            float u = add16(lo16(z[0]) * wa + lo16(z[1]) * wb, hi16(z[0]) * wa + hi16(z[1]) * wb);
            u += dpp<ROR4>(u);
            u += dpp<ROR8>(u);
            const unsigned p = cvtpk(u, u);
            B[2 * e] = SPARSE3(dppu<0x44>(p)); B[2 * e + 1] = SPARSE3(dppu<0xEE>(p));
        }
        v4f acc[3] = {{0.f, 0.f, 0.f, 0.f}, {0.f, 0.f, 0.f, 0.f}, {0.f, 0.f, 0.f, 0.f}};
#pragma unroll
        for (int i = 0; i < 4; ++i)
#pragma unroll
            for (int c = 0; c < 3; ++c) {
                acc[c] = mfma(4 * c + i < 9 ? S1[4 * c + i] : S3[4 * c + i - 9], B[i], acc[c]);
                if (i == 3) sm.red3[c][w][lane] = acc[c];
            }
        BARRIER();
        if (w < 3) {
            const float y = tile_sum<132>(ts);
            put(rw, op2, asu(y), T0, asu(dpp<X1>(y)), T1);
        }
        DRAIN();
        __builtin_amdgcn_endpgm();
    } else {
        steps<16 - WIN, 16>(STEP(t) { vstep(t_, Idx<DPLAN>{}); });
        sm.red2[w][lane] = o;
        BARRIER();
        PIN();
        if (isD) {
            __builtin_amdgcn_s_sleep(DSL);
            PIN();
            steps<DPLAN & 15, 5>(STEP(p) { constexpr int p = decltype(p_)::v; LDD(p); });
            PIN();
            FRESH();
            float bv[5];
#pragma unroll
            for (int i = 0; i < 5; ++i) bv[i] = ((const float*)sm.hb)[36 * w + 8 * i + (r & 6) + (lane & 1)];
            bv[4] = r < 4 ? bv[4] : 0.f;
            v4f acc = {0.f, 0.f, 0.f, 0.f};
#pragma unroll
            for (int i = 0; i < 5; ++i) { const unsigned p = cvtpk(bv[i], bv[i]); acc = mfma(S3[i], SPARSE3(p), acc); }
            sm.red3[0][w][lane] = acc;
            BARRIER();
            if (w == 0) {
                const float y = tile_sum<132>(ts);
                put(rw, op2, asu(y), T0, asu(dpp<X1>(y)), T1);
            }
        } else if (sp < 3 && w < 3) {
            const int oc = cg * 8 + (lane & 1) * 4 + (lane & 2);
            float xf = lo16(__builtin_amdgcn_raw_buffer_load_b16(mkrs(a.x, D * 2), oc, 0, 0));
            v2u g;
            asm volatile("" : "=v"(g), "+v"(xf));
            __builtin_amdgcn_s_sleep(PRE2);
            POLL1(g, og2);
            float s = asf(g.x) + dpp<X2>(asf(g.y));
            s += dpp<ROR4>(s);
            s += dpp<ROR8>(s);
            s = rbf(add16(s));
            const auto z = __builtin_amdgcn_permlane32_swap(asu(s), asu(s), false, false);
            const float y0 = xf + rbf(asf(z[0]) * rbf(silu(s)));
            __builtin_amdgcn_raw_buffer_store_b16((unsigned short)cvtpk(y0, y0), mkrs(pout, D * 2), (lane & 60) == 32 ? oc : OOB, 0, SC1);
            if (cg == 0) __builtin_amdgcn_raw_buffer_store_b64(v2u{T0, T1}, rw, lane == 0 ? 0 : OOB, 0, SC1);
            DRAIN();
        }
        __builtin_amdgcn_s_waitcnt(0x0f70);
        __builtin_amdgcn_endpgm();
    }
}

extern "C" void kernel_launch(void* const* d_in, const int* in_sizes, int n_in,
                              void* d_out, int out_size, void* d_ws, size_t ws_size,
                              hipStream_t stream) {
    Args a;
    a.x = (const char*)d_in[0]; a.k = (const char*)d_in[1]; a.v = (const char*)d_in[2];
    a.wq = (const char*)d_in[3]; a.wo = (const char*)d_in[4];
    a.wg = (const char*)d_in[6]; a.wu = (const char*)d_in[7]; a.wd = (const char*)d_in[8];
    a.out = (char*)d_out; a.ws = (char*)d_ws;
    fused_decode<<<256, 576, 0, stream>>>(a);
}
```

```cpp
#include <hip/hip_runtime.h>
#include <hip/hip_bf16.h>
#include <stdint.h>

typedef unsigned v4u __attribute__((ext_vector_type(4)));
typedef unsigned v8u __attribute__((ext_vector_type(8)));
typedef unsigned v2u __attribute__((ext_vector_type(2)));
typedef float v4f __attribute__((ext_vector_type(4)));
typedef float v2f __attribute__((ext_vector_type(2)));
typedef __bf16 bf16x2 __attribute__((ext_vector_type(2)));
typedef __bf16 bf16x8 __attribute__((ext_vector_type(8)));
typedef __amdgpu_buffer_rsrc_t rsrc_t;
#define LAS __attribute__((address_space(3)))

constexpr int WIN = 4;
constexpr int XP = 7, QCHK = 18;
constexpr int R0 = 10, RB = 31, TA = 134;
constexpr int N1 = 5, TB1 = 315, TB10 = 280;
constexpr int NADJ = 3, TL0 = 136;
constexpr int TX = 160, TQ = 573, TH = 867;
constexpr int QSL = 8, PRE = 8, PRE2 = 50, DSL = 4;
constexpr int OPLAN = 0x1233, DPLAN = 0x1234;

constexpr int D = 576, F = 4608;
constexpr int G1_OFF = 4096;
constexpr int ML_OFF = 57344;
constexpr int P1_OFF = 65536;
constexpr int P2_OFF = P1_OFF + 262144;
constexpr int WS_BYTES = P2_OFF + 144 * 1024;
constexpr int OOB = 0x7ffffff0;
constexpr int NT = 2, SC1 = 16;
constexpr unsigned SPIN = 1u << 16;
constexpr float LOG2E = 1.4426950408889634f;
constexpr float C2 = 0.088388347648318440f * LOG2E;

__host__ __device__ constexpr int map_h(int b) { return (b & 7) + 8 * (b >> 7); }
__host__ __device__ constexpr int map_sp(int b) { return (b >> 3) & 15; }

struct Args {
    const char *x, *wq, *wg, *wu, *k, *v, *wo, *wd;
    char *ws, *out;
};

__device__ __forceinline__ float asf(unsigned u) { return __uint_as_float(u); }
__device__ __forceinline__ unsigned asu(float f) { return __float_as_uint(f); }
__device__ __forceinline__ float lo16(unsigned u) { return asf(u << 16); }
__device__ __forceinline__ float hi16(unsigned u) { return asf(u & 0xffff0000u); }
__device__ __forceinline__ unsigned cvtpk(float lo, float hi) {
    return __builtin_bit_cast(unsigned, __builtin_convertvector(v2f{lo, hi}, bf16x2));
}
__device__ __forceinline__ float rbf(float x) { return lo16(cvtpk(x, x)); }
__device__ __forceinline__ v4f mfma(v4u a, v4u b, v4f c) {
    return __builtin_amdgcn_mfma_f32_16x16x32_bf16(__builtin_bit_cast(bf16x8, a), __builtin_bit_cast(bf16x8, b), c, 0, 0, 0);
}
template <int C> __device__ __forceinline__ float dpp(float v) { return asf(__builtin_amdgcn_mov_dpp(asu(v), C, 0xf, 0xf, true)); }
template <int C> __device__ __forceinline__ unsigned dppu(unsigned v) { return __builtin_amdgcn_mov_dpp(v, C, 0xf, 0xf, true); }
constexpr int X1 = 0xB1, X2 = 0x4E, HMIR = 0x141, MIR = 0x140, ROR4 = 0x124, ROR8 = 0x128;
__device__ __forceinline__ float add16(float a, float b) { auto r = __builtin_amdgcn_permlane16_swap(asu(a), asu(b), false, false); return asf(r[0]) + asf(r[1]); }
__device__ __forceinline__ float add16(float v) { return add16(v, v); }
__device__ __forceinline__ float add32(float v) { auto r = __builtin_amdgcn_permlane32_swap(asu(v), asu(v), false, false); return asf(r[0]) + asf(r[1]); }
__device__ __forceinline__ float max16(float v) { auto r = __builtin_amdgcn_permlane16_swap(asu(v), asu(v), false, false); return fmaxf(asf(r[0]), asf(r[1])); }
__device__ __forceinline__ float max32(float v) { auto r = __builtin_amdgcn_permlane32_swap(asu(v), asu(v), false, false); return fmaxf(asf(r[0]), asf(r[1])); }
__device__ __forceinline__ float sum8(float v) { v += dpp<X1>(v); v += dpp<X2>(v); v += dpp<HMIR>(v); return v; }
__device__ __forceinline__ float max8(float v) { v = fmaxf(v, dpp<X1>(v)); v = fmaxf(v, dpp<X2>(v)); return fmaxf(v, dpp<HMIR>(v)); }
__device__ __forceinline__ float sum64(float v) { v = sum8(v); v += dpp<MIR>(v); return add32(add16(v)); }
__device__ __forceinline__ float silu(float g) { return g * __builtin_amdgcn_rcpf(1.f + __builtin_amdgcn_exp2f(-LOG2E * g)); }
__device__ __forceinline__ rsrc_t mkrs(const void* p, int bytes) { return __builtin_amdgcn_make_buffer_rsrc((void*)p, 0, bytes, 0x00020000); }
template <int AUX> __device__ __forceinline__ v4u ld(rsrc_t r, int vo, int so = 0) { return __builtin_amdgcn_raw_buffer_load_b128(r, vo, so, AUX); }
__device__ __forceinline__ void put(rsrc_t r, int vo, unsigned a, unsigned t0, unsigned b, unsigned t1) {
    __builtin_amdgcn_raw_buffer_store_b128(v4u{a, t0, b, t1}, r, vo, 0, SC1);
}
template <typename T> __device__ __forceinline__ T sel(bool c, T a, T b) { return c ? a : b; }
template <int I> struct Idx { static constexpr int v = I; };
template <int I, int N, typename Fn> __device__ __forceinline__ void steps(Fn&& f) {
    if constexpr (I < N) { f(Idx<I>{}); steps<I + 1, N>(f); }
}
#define STEP(n) [&](auto n##_) __attribute__((always_inline))
#define USE(x) asm volatile("" ::"v"(x))
#define KEEPV(x) asm volatile("" : "+v"(x))
#define KEEPS(x) asm volatile("" : "+s"(x))
#define FRESH() asm volatile("" ::: "memory")
#define PIN() __builtin_amdgcn_sched_barrier(0)
#define BAD(g) (((g).y ^ T0) | ((g).w ^ T1))
#define ANY(x) __builtin_expect(__builtin_amdgcn_ballot_w64((x) != 0u) != 0ull, 0)

struct Smem {
    v4f red1[8][64], red2[8][64], red3[3][8][64];
    v4u xn[144];
    v4f hb[80];
    unsigned cnt[2][64];
    unsigned qf[64], xf[64];
    v2u q[64];
    v2f mw[2][8];
    float fw[2][8];
    v2u tag;
};

__device__ __forceinline__ void arrive(unsigned* c, int lane) {
    FRESH();
    __hip_atomic_fetch_add(c + lane, 1u, __ATOMIC_RELAXED, __HIP_MEMORY_SCOPE_WORKGROUP);
    FRESH();
}
__device__ __forceinline__ void await8(const unsigned* c, int lane) {
#pragma nounroll
    for (unsigned s = 0; s < SPIN * 64; ++s) {
        FRESH();
        const unsigned n = c[lane];
        FRESH();
        if (__builtin_amdgcn_readfirstlane(n) == 8) break;
        __builtin_amdgcn_s_sleep(1);
    }
}
#define BARRIER() asm volatile("s_waitcnt lgkmcnt(0)\n s_barrier" ::: "memory")
template <int STEP2> __device__ __forceinline__ float tile_sum(const float* p) {
    float a = 0.f, b = 0.f;
#pragma unroll
    for (int w = 0; w < 8; ++w) { a += p[w * 256]; b += p[w * 256 + STEP2]; }
    return a + b;
}
__device__ __forceinline__ unsigned now() { return (unsigned)__builtin_amdgcn_s_memrealtime(); }
__device__ __forceinline__ void until(unsigned t0, unsigned dt) {
#pragma nounroll
    for (unsigned i = 0; i < SPIN; ++i) {
        if (now() - t0 >= dt) break;
        __builtin_amdgcn_s_sleep(2);
    }
}

__global__ void __launch_bounds__(576, 3) __attribute__((aligned(4096))) fused_decode(Args a) {
    __shared__ Smem sm;
    asm volatile("" ::: "v160");
    unsigned long long t64 = __builtin_amdgcn_s_memrealtime();
    const int tid = threadIdx.x, lane = tid & 63, w = __builtin_amdgcn_readfirstlane(tid >> 6), b = blockIdx.x;
    const int h = map_h(b), sp = map_sp(b);
    const int r = lane >> 3, j = lane & 7;
    const bool isQ = sp < 4, has1 = sp <= 12, isO = !has1, isD = has1 && !isQ;
    const int gi = 9 * h + sp - 4;

    unsigned zc0 = 0, zc1 = 0;
    {
        unsigned long long pc; unsigned n;
        asm volatile("s_cmp_lg_u32 %5, 8\n s_cbranch_scc1 1f\n s_getpc_b64 %2\n.Lcode_here:\n s_mov_b32 %3, .Lcode_last-.Lcode_here\n"
                     "global_load_dword %0, %4, %2\n v_or_b32 %1, 0x2000, %4\n v_min_u32 %1, %3, %1\n global_load_dword %1, %1, %2\n1:"
                     : "+v"(zc0), "+v"(zc1), "=&s"(pc), "=&s"(n) : "v"(lane * 128), "s"(w) : "scc");
    }
    if (w == 8) {
        const rsrc_t rx = mkrs(a.x, D * 2), rw = mkrs(a.ws, WS_BYTES);
        unsigned zt = 0;
        if (isO) {
            const char *t = lane < 2 ? a.k : a.wq, *pf = sel(h < 8, a.wg, a.wu), *ps = sel(h < 8, (const char*)a.ws + 128, a.x);
            unsigned o = (b & 7) * 128u;
            if (sp == 15) {
                t = lane == 0 ? ps : lane < 3 ? a.wd : a.out;
                if (lane == 1 || lane == 2) o = (288u * h + 287u * (lane - 1)) * (D * 2);
            } else {
                if (sp == 14) t = lane < 2 ? a.v : pf;
                o += sel(sp == 13, (lane == 2 ? 256u : 575u) * 8192u, (lane == 2 ? 228u : lane == 3 ? 456u : 575u) * (F * 2));
                if (lane < 2) o = ((unsigned)h << 21) + lane * 0x1fff80u;
            }
            if (lane < sel(sp == 14, 5, 4)) zt = __builtin_nontemporal_load((const __attribute__((address_space(1))) unsigned*)(t + o));
            PIN();
        }
        KEEPS(t64);
        const unsigned tb = (unsigned)t64;
        sm.cnt[0][lane] = 0; sm.cnt[1][lane] = 0; sm.qf[lane] = 0; sm.xf[lane] = 0;
        BARRIER();
        PIN();
        until(tb, TX);
        PIN();
        const v4u xa = ld<0>(rx, lane * 16);
        const unsigned xb = __builtin_amdgcn_raw_buffer_load_b32(rx, 1024 + lane * 4, 0, 0);
        const v2u ep = __builtin_amdgcn_raw_buffer_load_b64(rw, 0, 0, SC1);
        PIN();
        const unsigned z = __builtin_amdgcn_raw_buffer_load_b32(rw, lane < 15 ? G1_OFF + (b * 15 + lane) * 128 : OOB, 0, SC1);
        PIN();

        const float e[10] = {lo16(xa.x), hi16(xa.x), lo16(xa.y), hi16(xa.y), lo16(xa.z), hi16(xa.z), lo16(xa.w), hi16(xa.w), lo16(xb), hi16(xb)};
        float s = 0.f;
#pragma unroll
        for (int i = 0; i < 10; ++i) s += e[i];
        const float mean = sum64(s) * (1.f / D);
        float c[10];
#pragma unroll
        for (int i = 0; i < 10; ++i) c[i] = e[i] - mean;
        float q = c[8] * c[8] + c[9] * c[9];
        q = lane < 32 ? q : 0.f;
#pragma unroll
        for (int i = 0; i < 8; ++i) q += c[i] * c[i];
        const float rs = __builtin_amdgcn_rsqf(sum64(q) * (1.f / D) + 1e-5f);
        unsigned n[10];
#pragma unroll
        for (int i = 0; i < 10; ++i) { const float t = c[i] * rs; n[i] = cvtpk(t, t); }
        sm.xn[2 * lane] = v4u{n[0], n[1], n[2], n[3]};
        sm.xn[2 * lane + 1] = v4u{n[4], n[5], n[6], n[7]};
        if (lane < 32) ((v2u*)&sm.xn[128])[lane] = v2u{n[8], n[9]};
        const unsigned T0 = __builtin_amdgcn_readfirstlane((ep.x + 0x9E3779B1u) | 1u), T1 = __builtin_amdgcn_readfirstlane(ep.y + 0x85EBCA77u);
        if (lane == 0) sm.tag = v2u{T0, T1};
        FRESH();
        sm.xf[lane] = 1;
        FRESH();

        USE(z); USE(zt);
        asm volatile("s_waitcnt vmcnt(0)" : "+v"(zc0), "+v"(zc1));

        v4u g;
        const int oq = G1_OFF + (64 * h + lane) * 16;
        bool asked = false;
        if (has1) {
#pragma nounroll
            for (unsigned i = 0; i < SPIN * 64; ++i) {
                FRESH();
                const unsigned n = sm.cnt[0][lane];
                FRESH();
                if (__builtin_amdgcn_readfirstlane(n) == 8) break;
                if (isD && !asked && now() - tb >= TQ) { g = ld<SC1>(rw, oq); asked = true; }
                __builtin_amdgcn_s_sleep(1);
            }
            const float y = tile_sum<160>((const float*)sm.red1 + ((lane >> 5) * 16 + j) * 4 + (r & 3));
            const unsigned pk = cvtpk(y, dpp<X1>(y));
            put(rw, (lane & 3) == 0 ? G1_OFF + (sel(isQ, 256 * h + 64 * sp, 4096 + gi * 64) + lane) * 4 : OOB, pk, T0, dppu<0xAA>(pk), T1);
        }
        if (!asked) { until(tb, TQ); g = ld<SC1>(rw, oq); }
#pragma nounroll
        for (unsigned i = 0; i < SPIN; ++i) {
            if (!ANY(BAD(g))) break;
            __builtin_amdgcn_s_sleep(QSL);
            FRESH();
            g = ld<SC1>(rw, oq);
        }
        sm.q[lane] = v2u{g.x, g.z};
        FRESH();
        sm.qf[lane] = 1;
        FRESH();

        await8(sm.cnt[1], lane);
        {
            const v2f mz = sm.mw[r & 1][j];
            const float M = max8(mz.x), f = __builtin_amdgcn_exp2f(mz.x - M);
            put(rw, (lane & 55) == 0 ? ML_OFF + ((h * 2 + r) * 16 + sp) * 16 : OOB, asu(M), T0, asu(sum8(f * mz.y)), T1);
            if (lane < 16) sm.fw[r][j] = f;
        }
        if (isD) {
            const int o0 = G1_OFF + (1024 + 72 * h + lane) * 16, o1 = lane < 8 ? o0 + 1024 : OOB;
            const unsigned lm = lane < 8 ? ~0u : 0u;
            v4u g0, u0, g1, u1;
            until(tb, TH);
#pragma nounroll
            for (unsigned i = 0; i < SPIN; ++i) {
                FRESH();
                g0 = ld<SC1>(rw, o0); u0 = ld<SC1>(rw, o0, (F / 4) * 16);
                g1 = ld<SC1>(rw, o1); u1 = ld<SC1>(rw, o1, (F / 4) * 16);
                if (!ANY(BAD(g0) | BAD(u0) | ((BAD(g1) | BAD(u1)) & lm))) break;
                __builtin_amdgcn_s_sleep(QSL);
            }
#define HID(g, u) v4f{rbf(silu(lo16(g.x))) * lo16(u.x), rbf(silu(hi16(g.x))) * hi16(u.x), rbf(silu(lo16(g.z))) * lo16(u.z), rbf(silu(hi16(g.z))) * hi16(u.z)}
            sm.hb[lane] = HID(g0, u0);
            if (lane < 16) sm.hb[64 + lane] = HID(g1, u1);
        }
        FRESH();
        const v4f fa = *(const v4f*)&sm.fw[r & 1][0], fb = *(const v4f*)&sm.fw[r & 1][4];
        int op1 = P1_OFF + ((h * 2 + (r & 1)) * 8 + (r & 6) + (j >> 2)) * 1024 + (sp * 4 + (j & 3)) * 16;
        KEEPV(op1);
        USE(fa); USE(fb);
        __builtin_amdgcn_s_setprio(3);
        BARRIER();
        v4f p[8];
#pragma unroll
        for (int i = 0; i < 8; ++i) p[i] = sm.red2[i][lane];
        PIN();
        v4f o = fa.x * p[0] + fa.y * p[1] + fa.z * p[2] + fa.w * p[3];
        o += fb.x * p[4] + fb.y * p[5] + fb.z * p[6] + fb.w * p[7];
        put(rw, op1, cvtpk(o.x, o.y), T0, cvtpk(o.z, o.w), T1);
        asm volatile(".Lcode_last:");
        __builtin_amdgcn_endpgm();
    }

    const bool isU = gi >= 72;
    const int c1 = sel(isQ, 4 * h + sp, sel(isU, gi - 72, gi));
    const int ld1 = sel(isQ, 8192, F * 2);
    const int kvb = w * 1024 + ((sp & 1) << 14 | (sp >> 1) << 18);
#define KST(t) (((t) & 1) << 13 | ((t) >> 1) << 15)
    v4u S1[9], K[16], V[16], S3[5];
#define LD1(i) S1[i] = (i) < 8 ? ld<NT>(r1, o1, ((i) & 3) * sa1 + ((i) >> 2) * sb1) : ld<NT>(r1, o1t)
#define LDK(t) K[t] = ld<NT>(rk, ok_, KST(t))
#define LDV(t) V[t] = ld<NT>(rv, ov, KST(t))
#define LDO(n) S3[n] = ld<NT>(r3, o3, ((n) + 1) * 2 * D * 2)
#define LDD(n) S3[n] = ld<NT>(r3, (n) < 4 ? o3 : o3x, (n) * 8 * D * 2)
#define ISSUE(p) do { if constexpr ((p) < 9) LD1(p); else if constexpr ((p) < 25) LDK((p) - 9); else LDV((p) - 25); } while (0)
#define LANDED(p) do { if constexpr ((p) < 9) USE(S1[p]); else if constexpr ((p) < 25) USE(K[(p) - 9]); else USE(V[(p) - 25]); } while (0)
#define WAITT(t, base) do { unsigned v_; \
        asm volatile("1: s_memrealtime vcc\n s_waitcnt lgkmcnt(0)\n s_sub_u32 %0, vcc_lo, %2\n s_cmp_lt_i32 %0, %1\n s_cbranch_scc1 1b" \
                     : "=&s"(v_) : "n"(t), "s"(base) : "scc", "vcc"); } while (0)
    const int orow = (32 * w + 8 * (r >> 1) + (r & 1)) * (D * 2), offo = 256 * h * (D * 2) + (sp - 13) * 384;
    int o1 = sel(has1, (8 * w + r) * ld1, orow) + j * 16, o1t = sel(has1, (512 + 8 * w + r) * ld1, orow + 256) + j * 16;
    int off1 = sel(has1, c1 * 128, offo), len1 = sel(has1, (D - 1) * ld1 + 128, 255 * D * 2 + 384);
    int pick1 = sel(isQ, 0, sel(isO, 3, sel(isU, 2, 1)));
    const int sa1 = sel(has1, 64 * ld1, 2 * D * 2), sb1 = sel(has1, 256 * ld1, 128);
    const char *pq = a.wq, *pg = a.wg, *pu = a.wu, *po = a.wo;
    asm volatile("" : "+v"(o1));
    asm volatile("" : "+s"(off1), "+s"(len1), "+s"(pick1));
    asm volatile("" : "+s"(pq), "+s"(pg), "+s"(pu), "+s"(po));
    const char* const p1 = sel(pick1 < 2, sel(pick1 == 0, pq, pg), sel(pick1 == 2, pu, po)) + (unsigned)off1;
    const rsrc_t r1 = mkrs(p1, len1);
    LD1(0);
    PIN();
    KEEPS(t64);
    unsigned tb = (unsigned)t64, tn = tb;
    int adj;
    asm volatile("s_and_b32 %0, %1, 0x380\n s_cmp_lg_u32 %0, %2\n s_cselect_b32 %0, -1, 0" : "=&s"(adj) : "s"((unsigned)(size_t)p1), "s"(sel(has1, 0x180, 1)) : "scc");
    PIN();

#define SCHED(n) (TA + ((n) <= N1 ? TB1 * (n) : TB1 * N1 + TB10 * ((n) - N1)) / 10)
#define PACE(n) do { unsigned v_; \
        asm volatile("s_cmp_ge_i32 %1, %2\n s_cbranch_scc1 2f\n1: s_memrealtime vcc\n s_waitcnt lgkmcnt(0)\n" \
                     "s_sub_u32 %0, vcc_lo, %3\n s_cmp_lt_i32 %0, %2\n s_cbranch_scc1 1b\n2:" \
                     : "=&s"(v_) : "s"(tn - tb), "n"(SCHED(n)), "s"(tb) : "scc", "vcc"); } while (0)
#define TICK() do { PIN(); tn = now(); } while (0)
#define ADJUST(n) do { unsigned d_; TICK(); \
        asm volatile("s_sub_u32 %1, %2, %0\n s_sub_u32 %1, %1, %3\n s_max_i32 %1, %1, 0\n s_and_b32 %1, %1, %4\n s_add_u32 %0, %0, %1" \
                     : "+s"(tb), "=&s"(d_) : "s"(tn), "n"(TL0 + (n) * RB), "s"(adj) : "scc"); } while (0)

    const rsrc_t rw = mkrs(a.ws, WS_BYTES);
    const rsrc_t rk = mkrs(a.k + ((unsigned)h << 21), 2 << 20);
    int ok_ = kvb + ((lane >> 2) & 3) * 256 + (lane >> 4) * 64 + (lane & 3) * 16;
    int off3 = sel(isD, 288 * h * (D * 2) + (sp - 4) * 128, offo);
    int len3 = sel(isO, 255 * D * 2 + 384, sel(isD, 287 * D * 2 + 128, 0));
    int o3 = sel(isD, (36 * w + r) * (D * 2) + j * 16, o1t);
    int o3x = r < 4 ? o3 : OOB;
    const int cg = 9 * h + 3 * sp + w;
    int ov = kvb + lane * 16;
    int og1 = P1_OFF + (h * 8 + w) * 2048 + lane * 16;
    const int pr = sel(isD, 16 + h, h);
    int op2 = ((lane ^ pr) & 1) != 0 ? OOB : P2_OFF + (sel(isD, sp - 4, 3 * sp - 39 + w) * 16 + (lane >> 2)) * 1024 + pr * 32 + (lane & 2) * 8;
    int og2 = P2_OFF + cg * 1024 + lane * 16;
    unsigned lowq = (lane & 4) ? 0u : ~0u, highq = ~lowq;
    unsigned msk[4];
#pragma unroll
    for (int k = 0; k < 4; ++k) { msk[k] = (j >> 1) == k ? ((lane & 1) ? 0xffff0000u : 0x0000ffffu) : 0u; KEEPV(msk[k]); }
#define SPARSE(pb) v4u{(pb) & msk[0], (pb) & msk[1], (pb) & msk[2], (pb) & msk[3]}
    KEEPS(off3); KEEPS(len3);
    KEEPV(o1t); KEEPV(ok_); KEEPV(o3); KEEPV(o3x); KEEPV(ov); KEEPV(og1); KEEPV(op2); KEEPV(og2); KEEPV(lowq); KEEPV(highq);
    PIN();
    steps<1, WIN>(STEP(p) { constexpr int p = decltype(p_)::v; WAITT(R0 + p * RB, tb); PIN(); LD1(p); PIN(); });
    const char *pv = a.v, *pd = a.wd, *pout = a.out;
    asm volatile("" : "+s"(pv), "+s"(pd));
    const char* p3 = sel(isD, pd, po) + (unsigned)off3;
    KEEPS(p3);
    const rsrc_t rv = mkrs(pv + ((unsigned)h << 21), 2 << 20);
    const rsrc_t r3 = mkrs(p3, len3);
    PIN();
    BARRIER();
    PIN();

    static_assert(WIN >= 4 && WIN <= 6 && XP < 9 && QCHK - WIN + 9 > 9, "schedule");
    const v4u* const qs = (const v4u*)sm.q + (r & 1) * 16 + (lane >> 4) * 4 + (lane & 3);
    v4u qv, qe, qo;
    v2u tg;
    unsigned xn[9], xok, qok, T0, T1;
    float sc[8], ta[8], tb_[8];
    v4f acc1 = {0.f, 0.f, 0.f, 0.f};
#define LDX() do { FRESH(); xok = sm.xf[lane]; FRESH(); \
        _Pragma("unroll") for (int i = 0; i < 9; ++i) xn[i] = ((const unsigned*)sm.xn)[64 * i + 8 * w + r]; \
        tg = sm.tag; FRESH(); } while (0)
    steps<0, 25>(STEP(n) {
        constexpr int n = decltype(n_)::v, QN = QCHK - WIN + 9;
        if constexpr (n == XP) LDX();
        if constexpr (n == QN) { FRESH(); qok = sm.qf[lane]; FRESH(); qv = *qs; FRESH(); }
        LANDED(n);
        if constexpr (n < NADJ) ADJUST(n);
        PACE(n);
        PIN();
        ISSUE(n + WIN);
        TICK();
        PIN();
        if constexpr (n == XP) {
            if (__builtin_expect(__builtin_amdgcn_readfirstlane(xok) == 0, 0)) {
#pragma nounroll
                for (unsigned s = 0; s < SPIN * 64; ++s) {
                    __builtin_amdgcn_s_sleep(1);
                    LDX();
                    if (__builtin_amdgcn_readfirstlane(xok) != 0) break;
                }
            }
            T0 = __builtin_amdgcn_readfirstlane(tg.x); T1 = __builtin_amdgcn_readfirstlane(tg.y);
            PIN();
        }
        if constexpr (n >= XP && n < 9) {
            constexpr int lo = n == XP ? 0 : n, hi = n + 1;
#pragma unroll
            for (int i = 0; i < 9; ++i) if (i >= lo && i < hi) { acc1 = mfma(S1[i], SPARSE(xn[i]), acc1); PIN(); }
        }
        if constexpr (n == 9) {
            sm.red1[w][lane] = acc1;
            arrive(sm.cnt[0], lane);
            PIN();
        }
        if constexpr (n == QN) {
            if (__builtin_expect(__builtin_amdgcn_readfirstlane(qok) == 0, 0)) {
#pragma nounroll
                for (unsigned s = 0; s < SPIN * 64; ++s) {
                    __builtin_amdgcn_s_sleep(1);
                    FRESH(); qok = sm.qf[lane]; FRESH(); qv = *qs; FRESH();
                    if (__builtin_amdgcn_readfirstlane(qok) != 0) break;
                }
            }
            qe = qv & lowq; qo = qv & highq;
        }
        if constexpr (n >= QN) {
            constexpr int NS = 25 - QN, lo = 8 * (n - QN) / NS, hi = 8 * (n + 1 - QN) / NS;
            v4f d[8];
#pragma unroll
            for (int p = 0; p < 8; ++p) if (p >= lo && p < hi) { d[p] = mfma(K[2 * p], qe, v4f{0.f, 0.f, 0.f, 0.f}); PIN(); }
#pragma unroll
            for (int p = 0; p < 8; ++p) if (p >= lo && p < hi) { d[p] = mfma(K[2 * p + 1], qo, d[p]); PIN(); }
#pragma unroll
            for (int p = 0; p < 8; ++p) if (p >= lo && p < hi) { ta[p] = d[p].x + dpp<X1>(d[p].y); tb_[p] = d[p].z + dpp<X1>(d[p].w); }
            PIN();
#pragma unroll
            for (int p = 0; p < 8; ++p) if (p >= lo && p < hi) sc[p] = ta[p] + dpp<X2>(tb_[p]);
            PIN();
        }
    });
    float m = fmaxf(fmaxf(fmaxf(sc[0], sc[1]), fmaxf(sc[2], sc[3])), fmaxf(fmaxf(sc[4], sc[5]), fmaxf(sc[6], sc[7])));
    m = dpp<0>(m);
    m = fmaxf(m, dpp<HMIR>(m));
    m = max32(max16(m)) * C2;
    unsigned pb[8];
    float l = 0.f;
#define EXPS(a, b) do { _Pragma("unroll") for (int i = a; i < b; ++i) { \
        const float p = __builtin_amdgcn_exp2f(dpp<0>(sc[i] * C2 - m)); pb[i] = cvtpk(p, p); l += p; } } while (0)
    PIN();

    v4f o = {0.f, 0.f, 0.f, 0.f};
    unsigned mo[4];
    int oml;
    auto vstep = [&](auto t_, auto plan_) __attribute__((always_inline)) {
        constexpr int t = decltype(t_)::v, plan = decltype(plan_)::v;
        v4u B;
#define MKB() do { const unsigned pn = pb[t >> 1], pf = dppu<HMIR>(pn); \
        B = (t & 1) ? v4u{pf & msk[0], pf & msk[1], pn & msk[2], pn & msk[3]} : v4u{pn & msk[0], pn & msk[1], pf & msk[2], pf & msk[3]}; } while (0)
        if constexpr (t > 0) MKB();
        LANDED(25 + t);
        if constexpr (t + WIN < 19) PACE(25 + t);
        PIN();
        if constexpr (t + WIN < 16) ISSUE(25 + t + WIN);
        else {
            constexpr int lo = t + WIN == 16 ? 0 : (plan >> (4 * (16 - t))) & 15, hi = (plan >> (4 * (15 - t))) & 15;
            steps<lo, hi>(STEP(p) { constexpr int p = decltype(p_)::v; if constexpr (plan >> 24) LDO(p); else LDD(p); });
        }
        if constexpr (t + WIN < 18) TICK();
        PIN();
        if constexpr (t == 0) { EXPS(0, 4); MKB(); }
        o = mfma(V[t], B, o);
        if constexpr (t == 1) {
            EXPS(4, 8);
            l += dpp<HMIR>(l);
            l = add32(add16(l));
            if ((lane & 55) == 0) sm.mw[r][w] = v2f{m, l};
            arrive(sm.cnt[1], lane);
        }
        if constexpr (t == 6) {
#pragma unroll
            for (int k = 0; k < 4; ++k) { mo[k] = ((lane >> 2) & 3) == k ? ((lane & 2) ? 0xffff0000u : 0x0000ffffu) : 0u; KEEPV(mo[k]); }
            oml = ML_OFF + (h * 2 + (w >> 2)) * 256 + (lane & 60) * 4;
            KEEPV(oml);
        }
        PIN();
    };
    steps<0, 16 - WIN>(STEP(t) { vstep(t_, Idx<0>{}); });

#define SPARSE3(pb) v4u{(pb) & mo[0], (pb) & mo[1], (pb) & mo[2], (pb) & mo[3]}
#define ASK(a, d, o) "buffer_load_dwordx4 v[" #a ":" #d "], %[p], %[rs], 0 offen offset:" #o " sc1\n"
#define TAGS(m, b, d) "v_cmp_eq_u32_e64 " m ", %[t0], v" #b "\n v_cmp_eq_u32_e64 %[m2], %[t1], v" #d "\n s_and_b64 " m ", " m ", %[m2]\n"
#define KEEP(x, y, a, c) "v_cndmask_b32_e32 %[" #x "], %[" #x "], v" #a ", vcc\n v_cndmask_b32_e32 %[" #y "], %[" #y "], v" #c ", vcc\n"
#define LEAVE "s_andn2_b64 exec, exec, vcc\n s_cbranch_scc0 2f\n"
#define ASK2 ASK(136, 139, 0) ASK(140, 143, 1024)
#define ENTER "s_mov_b32 %[c], %[spin]\n1:\n s_waitcnt vmcnt(0)\n"
#define ROUND "s_sub_u32 %[c], %[c], 1\n s_cbranch_scc0 1b\n2: s_mov_b64 exec, -1"
#define POLL_IN(p_) [p] "v"(p_), [rs] "s"(dw), [t0] "s"(T0), [t1] "s"(T1), [spin] "n"(SPIN)
#define POLL1(g_, p_) do { unsigned long long m2_; unsigned c_; \
        asm volatile(ASK(136, 139, 0) ENTER TAGS("vcc", 137, 139) KEEP(gx, gy, 136, 138) LEAVE ASK(136, 139, 0) ROUND \
                     : [gx] "+v"(g_.x), [gy] "+v"(g_.y), [m2] "=&s"(m2_), [c] "=&s"(c_), "=&{v[136:143]}"(xa) \
                     : POLL_IN(p_) : "vcc", "scc", "memory"); \
    } while (0)
#define POLL2(ga, gb, p_) do { unsigned long long m_, m2_; unsigned c_; \
        asm volatile(ENTER TAGS("%[m]", 137, 139) TAGS("vcc", 141, 143) "s_and_b64 vcc, vcc, %[m]\n" \
                     KEEP(gx, gy, 136, 138) KEEP(gz, gw, 140, 142) LEAVE ASK2 ROUND \
                     : [gx] "+v"(ga.x), [gy] "+v"(ga.y), [gz] "+v"(gb.x), [gw] "+v"(gb.y), [m] "=&s"(m_), [m2] "=&s"(m2_), [c] "=&s"(c_), "+{v[136:143]}"(xa) \
                     : POLL_IN(p_) : "vcc", "scc", "memory"); \
    } while (0)
#define DRAIN() asm volatile("s_waitcnt vmcnt(0)" ::"{v[136:143]}"(xa) : "memory")
    v8u xa;
    const v4u dw = {(unsigned)(size_t)a.ws, (unsigned)((size_t)a.ws >> 32) & 0xffffu, (unsigned)WS_BYTES, 0x00020000u};
    const float* const ts = (const float*)sm.red3[w] + ((lane >> 5) * 16 + 2 * j) * 4 + (r & 3);
    if (isO) {
        steps<16 - WIN, 16>(STEP(t) { vstep(t_, Idx<0x1000000 | OPLAN>{}); });
        sm.red2[w][lane] = o;
        BARRIER();
        PIN();
        steps<OPLAN & 15, 3>(STEP(p) { constexpr int p = decltype(p_)::v; LDO(p); });
        PIN();
        v4u ml = ld<SC1>(rw, oml);
        __builtin_amdgcn_s_sleep(PRE);
        if (ANY(BAD(ml))) {
#pragma nounroll
            for (unsigned s = 0; s < SPIN; ++s) {
                __builtin_amdgcn_s_sleep(QSL);
                FRESH();
                ml = ld<SC1>(rw, oml);
                if (!ANY(BAD(ml))) break;
            }
        }
        USE(S3[0]); USE(S3[1]); USE(S3[2]);
        PIN();
        asm volatile(ASK2 : "=&{v[136:143]}"(xa) : [p] "v"(og1), [rs] "s"(dw) : "memory");
        PIN();
        float M = fmaxf(asf(ml.x), dpp<ROR4>(asf(ml.x)));
        M = max32(max16(fmaxf(M, dpp<ROR8>(M))));
        const float e = __builtin_amdgcn_exp2f(asf(ml.x) - M);
        float L = e * asf(ml.z);
        L += dpp<ROR4>(L);
        L += dpp<ROR8>(L);
        const float wt = e * __builtin_amdgcn_rcpf(add32(add16(L)));
        const auto wz = __builtin_amdgcn_permlane32_swap(asu(wt), asu(wt), false, false);
        float wa = asf(wz[0]), wb = asf(wz[1]);
        v2u g[2];
        asm volatile("" : "=v"(g[0]), "=v"(g[1]), "+v"(wa), "+v"(wb));
        PIN();
        POLL2(g[0], g[1], og1);
        v4u B[4];
#pragma unroll
        for (int e = 0; e < 2; ++e) {
            const auto z = __builtin_amdgcn_permlane32_swap(g[e].x, g[e].y, false, false);
            float u = add16(lo16(z[0]) * wa + lo16(z[1]) * wb, hi16(z[0]) * wa + hi16(z[1]) * wb);
            u += dpp<ROR4>(u);
            u += dpp<ROR8>(u);
            const unsigned p = cvtpk(u, u);
            B[2 * e] = SPARSE3(dppu<0x44>(p)); B[2 * e + 1] = SPARSE3(dppu<0xEE>(p));
        }
        v4f acc[3] = {{0.f, 0.f, 0.f, 0.f}, {0.f, 0.f, 0.f, 0.f}, {0.f, 0.f, 0.f, 0.f}};
#pragma unroll
        for (int i = 0; i < 4; ++i)
#pragma unroll
            for (int c = 0; c < 3; ++c) {
                acc[c] = mfma(4 * c + i < 9 ? S1[4 * c + i] : S3[4 * c + i - 9], B[i], acc[c]);
                if (i == 3) sm.red3[c][w][lane] = acc[c];
            }
        BARRIER();
        if (w < 3) {
            const float y = tile_sum<132>(ts);
            put(rw, op2, asu(y), T0, asu(dpp<X1>(y)), T1);
        }
        DRAIN();
        __builtin_amdgcn_endpgm();
    } else {
        steps<16 - WIN, 16>(STEP(t) { vstep(t_, Idx<DPLAN>{}); });
        sm.red2[w][lane] = o;
        BARRIER();
        PIN();
        if (isD) {
            __builtin_amdgcn_s_sleep(DSL);
            PIN();
            steps<DPLAN & 15, 5>(STEP(p) { constexpr int p = decltype(p_)::v; LDD(p); });
            PIN();
            FRESH();
            float bv[5];
#pragma unroll
            for (int i = 0; i < 5; ++i) bv[i] = ((const float*)sm.hb)[36 * w + 8 * i + (r & 6) + (lane & 1)];
            bv[4] = r < 4 ? bv[4] : 0.f;
            v4f acc = {0.f, 0.f, 0.f, 0.f};
#pragma unroll
            for (int i = 0; i < 5; ++i) { const unsigned p = cvtpk(bv[i], bv[i]); acc = mfma(S3[i], SPARSE3(p), acc); }
            sm.red3[0][w][lane] = acc;
            BARRIER();
            if (w == 0) {
                const float y = tile_sum<132>(ts);
                put(rw, op2, asu(y), T0, asu(dpp<X1>(y)), T1);
            }
        } else if (sp < 3 && w < 3) {
            const int oc = cg * 8 + (lane & 1) * 4 + (lane & 2);
            float xf = lo16(__builtin_amdgcn_raw_buffer_load_b16(mkrs(a.x, D * 2), oc, 0, 0));
            v2u g;
            asm volatile("" : "=v"(g), "+v"(xf));
            __builtin_amdgcn_s_sleep(PRE2);
            POLL1(g, og2);
            float s = asf(g.x) + dpp<X2>(asf(g.y));
            s += dpp<ROR4>(s);
            s += dpp<ROR8>(s);
            s = rbf(add16(s));
            const auto z = __builtin_amdgcn_permlane32_swap(asu(s), asu(s), false, false);
            const float y0 = xf + rbf(asf(z[0]) * rbf(silu(s)));
            __builtin_amdgcn_raw_buffer_store_b16((unsigned short)cvtpk(y0, y0), mkrs(pout, D * 2), (lane & 60) == 32 ? oc : OOB, 0, SC1);
            if (cg == 0) __builtin_amdgcn_raw_buffer_store_b64(v2u{T0, T1}, rw, lane == 0 ? 0 : OOB, 0, SC1);
            DRAIN();
        }
        __builtin_amdgcn_s_waitcnt(0x0f70);
        __builtin_amdgcn_endpgm();
    }
}

extern "C" void kernel_launch(void* const* d_in, const int* in_sizes, int n_in,
                              void* d_out, int out_size, void* d_ws, size_t ws_size,
                              hipStream_t stream) {
    Args a;
    a.x = (const char*)d_in[0]; a.k = (const char*)d_in[1]; a.v = (const char*)d_in[2];
    a.wq = (const char*)d_in[3]; a.wo = (const char*)d_in[4];
    a.wg = (const char*)d_in[6]; a.wu = (const char*)d_in[7]; a.wd = (const char*)d_in[8];
    a.out = (char*)d_out; a.ws = (char*)d_ws;
    fused_decode<<<256, 576, 0, stream>>>(a);
}
```
